```python
import torch

def _softmax(x, axis=-1):
    return torch.softmax(x, dim=axis)

def _gelu(x):
    return torch.nn.functional.gelu(x, approximate='tanh')

def _silu(x):
    return torch.nn.functional.silu(x)

def _sigmoid(x):
    return torch.sigmoid(x)

def _layernorm(x, axis=-1, eps=1e-05):
    m = x.mean(dim=axis, keepdim=True)
    v = x.var(dim=axis, keepdim=True, unbiased=False)
    return (x - m) / torch.sqrt(v + eps)

def _scatter_add(tgt, idx, src, axis=0):
    return tgt.clone().index_add_(axis, idx, src)

def _segment_sum(src, seg, n):
    out = torch.zeros((n,) + src.shape[1:], dtype=src.dtype)
    return out.index_add_(0, seg, src)

def _pad(x, pw):
    flat = tuple((v for pair in reversed(pw) for v in pair))
    return torch.nn.functional.pad(x, flat)
kernel_name = 'adaLN_cond__ssm_conv_s4d__glu_gated_swiglu__prompt'

def setup_inputs():

    _w = lambda a: (lambda t: t.to(torch.bfloat16) if t.is_floating_point() else t)(torch.as_tensor(a))

    def _A_setup():
        Bsz, L, D, N = (1, 2048, 448, 256)
        return {'u': _w((lambda *s: torch.randn(*s, dtype=torch.bfloat16) * 0.05)(Bsz, L, D)), 'log_A_real': _w((lambda *s: torch.randn(*s, dtype=torch.bfloat16) * 3.2)(D, N) * 0.5), 'A_imag': _w((lambda *s: torch.randn(*s, dtype=torch.bfloat16) * 0.05)(D, N) * 3.14), 'C_real': _w((lambda *s: torch.randn(*s, dtype=torch.bfloat16) * 0.2)(D, N) / N ** 0.5), 'C_imag': _w((lambda *s: torch.randn(*s, dtype=torch.bfloat16) * 0.2)(D, N) / N ** 0.5), 'log_dt': _w((lambda *s: torch.randn(*s, dtype=torch.bfloat16) * 0.05)(D) * 0.5 - 4.0), 'D_skip': _w((lambda *s: torch.randn(*s, dtype=torch.bfloat16) * 0.05)(D))}
    _A_in = _A_setup()

    def _B_setup():
        B, S, D, F = (1, 2048, 448, 1792)
        return {'x': _w((lambda *s: torch.randn(*s, dtype=torch.bfloat16) * 0.05)(B, S, D)), 'w1': _w((lambda *s: torch.randn(*s, dtype=torch.bfloat16) * 0.4)(D, F) * (2.0 / D) ** 0.5), 'w3': _w((lambda *s: torch.randn(*s, dtype=torch.bfloat16) * 0.4)(D, F) * (2.0 / D) ** 0.5), 'w2': _w((lambda *s: torch.randn(*s, dtype=torch.bfloat16) * 0.4)(F, D) * (2.0 / F) ** 0.5)}
    _B_in = _B_setup()
    return {'A_u': _A_in['u'], 'A_log_A_real': _A_in['log_A_real'], 'A_A_imag': _A_in['A_imag'], 'A_C_real': _A_in['C_real'], 'A_C_imag': _A_in['C_imag'], 'A_log_dt': _A_in['log_dt'], 'A_D_skip': _A_in['D_skip'], 'B_x': _B_in['x'], 'B_w1': _B_in['w1'], 'B_w3': _B_in['w3'], 'B_w2': _B_in['w2'], 'c': _w((lambda *s: torch.randn(*s, dtype=torch.bfloat16) * 0.4)(1, 448) * 8.0), 'w_mod': _w((lambda *s: torch.randn(*s, dtype=torch.bfloat16) * 0.4)(448, 6 * 448) * 448 ** (-0.5) * 8.0)}

def reference(A_u, A_log_A_real, A_A_imag, A_C_real, A_C_imag, A_log_dt, A_D_skip, B_x, B_w1, B_w3, B_w2, c, w_mod):

    def _A_ref(u, log_A_real, A_imag, C_real, C_imag, log_dt, D_skip):
        L = u.shape[1]
        dt = torch.exp(log_dt)
        A = -torch.exp(log_A_real) + 1j * A_imag
        C = (C_real + 1j * C_imag) * dt[:, None]
        pos = (lambda x, dt: x.to(dt))(torch.arange(L), log_dt.dtype)
        P = torch.exp((A * dt[:, None])[:, :, None] * pos[None, None, :])
        K = 2.0 * torch.einsum('dn,dnl->dl', C, P).real
        L2 = 2 * L
        u_t = (lambda x, *d: x.permute(*d))(u, 0, 2, 1)
        Kf = (lambda x, *a, **k: torch.fft.rfft(x.float(), *a, **k))(K, L2)
        Uf = (lambda x, *a, **k: torch.fft.rfft(x.float(), *a, **k))(u_t, L2)
        y = (lambda x, *a, **k: torch.fft.irfft(x, *a, **k).to(torch.bfloat16))(Uf * Kf[None, :, :], L2)[:, :, :L]
        y = (lambda x, *d: x.permute(*d))(y, 0, 2, 1)
        return y + u * D_skip[None, None, :]

    def _B_ref(x, w1, w3, w2):
        gate = torch.einsum('bsd,df->bsf', x, w1)
        up = torch.einsum('bsd,df->bsf', x, w3)
        h = _silu(gate) * up
        out = torch.einsum('bsf,fd->bsd', h, w2)
        return out
    x = A_u
    mod = (_silu(c) @ w_mod).reshape(c.shape[0], 6, 1, -1)
    sa, ca, ga, sb, cb, gb = (mod[:, 0].clone(), mod[:, 1].clone(), mod[:, 2].clone(), mod[:, 3].clone(), mod[:, 4].clone(), mod[:, 5].clone())
    h = _layernorm(x, axis=-1) * (1 + ca) + sa
    x = x + ga * _A_ref(h, A_log_A_real, A_A_imag, A_C_real, A_C_imag, A_log_dt, A_D_skip)
    h = _layernorm(x, axis=-1) * (1 + cb) + sb
    x = x + gb * _B_ref(h, B_w1, B_w3, B_w2)
    return x
```

```cpp
#include <hip/hip_runtime.h>
#include <hip/hip_bf16.h>
#include <stdint.h>

typedef unsigned short u16;
#define L_ 2048
#define D_ 448
#define N_ 256
#define F_ 1792

__device__ __forceinline__ float bf2f(u16 v){ return __uint_as_float(((unsigned)v)<<16); }
__device__ __forceinline__ u16 f2bf(float f){ __hip_bfloat16 b = __float2bfloat16(f); return *(u16*)&b; }
__device__ __forceinline__ float rb(float f){ return bf2f(f2bf(f)); }

__global__ void k_mod(const u16* c, const u16* wm, float* mod){
  int j = blockIdx.x*blockDim.x+threadIdx.x;
  if(j>=6*D_) return;
  float acc=0;
  for(int i=0;i<D_;i++){ float x=bf2f(c[i]); float s=rb(x/(1.f+__expf(-x))); acc+=s*bf2f(wm[i*6*D_+j]); }
  mod[j]=rb(acc);
}
__global__ void k_ln(const u16* x, const float* sh, const float* sc, u16* out){
  int l=blockIdx.x; int t=threadIdx.x;
  float v[7]; float s=0;
  for(int i=0;i<7;i++){ v[i]=bf2f(x[l*D_+t+64*i]); s+=v[i]; }
  for(int o=32;o;o>>=1) s+=__shfl_xor(s,o);
  float m=s/D_; float q=0;
  for(int i=0;i<7;i++){ float d=v[i]-m; q+=d*d; }
  for(int o=32;o;o>>=1) q+=__shfl_xor(q,o);
  float var=rb(q/D_); m=rb(m);
  float sd=rb(sqrtf(rb(var+1e-5f)));
  for(int i=0;i<7;i++){ int d=t+64*i; float n=rb(rb(v[i]-m)/sd); float h=rb(rb(n*rb(1.f+sc[d]))+sh[d]); out[l*D_+d]=f2bf(h); }
}
__global__ void k_kgen(const u16* lar,const u16* aim,const u16* cr,const u16* ci,const u16* ldt,float* K){
  int d=blockIdx.y; int l=blockIdx.x*blockDim.x+threadIdx.x;
  float dt=rb(expf(bf2f(ldt[d])));
  float pos=rb((float)l);
  float acc=0;
  for(int n=0;n<N_;n++){
    float ar=-rb(expf(bf2f(lar[d*N_+n])))*dt, ai=bf2f(aim[d*N_+n])*dt;
    float c0=bf2f(cr[d*N_+n])*dt, c1=bf2f(ci[d*N_+n])*dt;
    float e=expf(ar*pos); float sn,cs; sincosf(ai*pos,&sn,&cs);
    acc+=e*(c0*cs-c1*sn);
  }
  K[d*L_+l]=2.f*acc;
}
__global__ void k_conv(const u16* x,const u16* h,const float* K,const u16* dsk,const float* ga,u16* x1){
  int d=blockIdx.y; int l=blockIdx.x*blockDim.x+threadIdx.x;
  float acc=0;
  for(int j=0;j<=l;j++) acc+=K[d*L_+j]*bf2f(h[(l-j)*D_+d]);
  float y=rb(acc);
  float a=rb(y+rb(bf2f(h[l*D_+d])*bf2f(dsk[d])));
  x1[l*D_+d]=f2bf(bf2f(x[l*D_+d])+rb(ga[d]*a));
}
__global__ void k_gu(const u16* h,const u16* w1,const u16* w3,u16* g){
  int l=blockIdx.y; int f=blockIdx.x*blockDim.x+threadIdx.x;
  float a=0,b=0;
  for(int k=0;k<D_;k++){ float x=bf2f(h[l*D_+k]); a+=x*bf2f(w1[k*F_+f]); b+=x*bf2f(w3[k*F_+f]); }
  a=rb(a); b=rb(b);
  float s=rb(a/(1.f+__expf(-a)));
  g[l*F_+f]=f2bf(s*b);
}
__global__ void k_dn(const u16* g,const u16* w2,const u16* x1,const float* gb,u16* out){
  int l=blockIdx.y; int d=blockIdx.x*blockDim.x+threadIdx.x;
  float a=0;
  for(int k=0;k<F_;k++) a+=bf2f(g[l*F_+k])*bf2f(w2[k*D_+d]);
  a=rb(a);
  out[l*D_+d]=f2bf(bf2f(x1[l*D_+d])+rb(gb[d]*a));
}

extern "C" void kernel_launch(void* const* d_in, const int* in_sizes, int n_in,
                              void* d_out, int out_size, void* d_ws, size_t ws_size,
                              hipStream_t stream) {
  const u16* u=(const u16*)d_in[0]; const u16* lar=(const u16*)d_in[1]; const u16* aim=(const u16*)d_in[2];
  const u16* cr=(const u16*)d_in[3]; const u16* ci=(const u16*)d_in[4]; const u16* ldt=(const u16*)d_in[5];
  const u16* dsk=(const u16*)d_in[6]; const u16* w1=(const u16*)d_in[8]; const u16* w3=(const u16*)d_in[9];
  const u16* w2=(const u16*)d_in[10]; const u16* c=(const u16*)d_in[11]; const u16* wm=(const u16*)d_in[12];
  char* ws=(char*)d_ws;
  float* mod=(float*)ws; ws+=16384;
  u16* h=(u16*)ws; ws+=L_*D_*2;
  float* K=(float*)ws; ws+=L_*D_*4;
  u16* x1=(u16*)ws; ws+=L_*D_*2;
  u16* h2=(u16*)ws; ws+=L_*D_*2;
  u16* g=(u16*)ws; ws+=L_*F_*2;
  k_mod<<<(6*D_+63)/64,64,0,stream>>>(c,wm,mod);
  k_ln<<<L_,64,0,stream>>>(u,mod+0*D_,mod+1*D_,h);
  k_kgen<<<dim3(L_/256,D_),256,0,stream>>>(lar,aim,cr,ci,ldt,K);
  k_conv<<<dim3(L_/256,D_),256,0,stream>>>(u,h,K,dsk,mod+2*D_,x1);
  k_ln<<<L_,64,0,stream>>>(x1,mod+3*D_,mod+4*D_,h2);
  k_gu<<<dim3(F_/64,L_),64,0,stream>>>(h2,w1,w3,g);
  k_dn<<<dim3(D_/64,L_),64,0,stream>>>(g,w2,x1,mod+5*D_,(u16*)d_out);
}
```

```cpp
#include <hip/hip_runtime.h>
#include <stdint.h>

typedef unsigned short u16;
typedef unsigned int u32;
typedef __attribute__((ext_vector_type(8))) __bf16 bf16x8;
typedef __attribute__((ext_vector_type(2))) __bf16 bf16x2;
typedef __attribute__((ext_vector_type(4))) short s16x4;
typedef __attribute__((ext_vector_type(2))) float f32x2;
typedef __attribute__((ext_vector_type(4))) float f32x4;
typedef __attribute__((ext_vector_type(16))) float f32x16;
typedef __attribute__((ext_vector_type(4))) u32 u32x4;
typedef __attribute__((ext_vector_type(2))) u32 u32x2;
#define LAS __attribute__((address_space(3)))
#define DEV __device__ __forceinline__

constexpr int L_ = 2048, D_ = 448, N_ = 256, F_ = 1792;
constexpr int KR_STRIDE = 2064;
constexpr size_t OFF_MOD  = 0;
constexpr size_t OFF_ST1  = 16384;
constexpr size_t OFF_XP   = OFF_ST1 + 16384;
constexpr size_t OFF_KR   = OFF_XP + (size_t)224 * 2048 * 4;
constexpr size_t OFF_WT   = OFF_KR + (size_t)448 * KR_STRIDE * 2;
constexpr size_t OFF_PART = OFF_WT + (size_t)16 * 29 * 7 * 1024;
constexpr size_t OFF_W2T  = OFF_PART + (size_t)28 * 2 * 3584 * 4;
constexpr size_t OFF_X1T  = OFF_W2T + (size_t)14 * 112 * 1024;
constexpr size_t OFF_H    = OFF_X1T + (size_t)448 * 2048 * 2;

DEV u32 pack2(float lo, float hi) { f32x2 v = {lo, hi}; bf16x2 b = __builtin_convertvector(v, bf16x2); return __builtin_bit_cast(u32, b); }
DEV float lo_f(u32 u) { return __uint_as_float(u << 16); }
DEV float hi_f(u32 u) { return __uint_as_float(u & 0xffff0000u); }
DEV float bf2f(u16 v) { return __uint_as_float(((u32)v) << 16); }
DEV float rb(float f) { return lo_f(pack2(f, f)); }
DEV float dot2(u32 a, u32 b, float c) { return __builtin_amdgcn_fdot2_f32_bf16(__builtin_bit_cast(bf16x2, a), __builtin_bit_cast(bf16x2, b), c, false); }
DEV bf16x8 asb(u32x4 v) { return __builtin_bit_cast(bf16x8, v); }
DEV float fexp(float x) { return __builtin_amdgcn_exp2f(x * 1.44269504088896341f); }

template <int CTRL, int RMASK> DEV float dpp_add(float v) {
  int t = __builtin_amdgcn_update_dpp(0, __float_as_int(v), CTRL, RMASK, 0xf, false);
  return v + __int_as_float(t);
}
DEV float wave_sum(float v) {
  v = dpp_add<0x111, 0xf>(v); v = dpp_add<0x112, 0xf>(v); v = dpp_add<0x114, 0xf>(v); v = dpp_add<0x118, 0xf>(v);
  v = dpp_add<0x142, 0xa>(v); v = dpp_add<0x143, 0xc>(v);
  return __int_as_float(__builtin_amdgcn_readlane(__float_as_int(v), 63));
}

struct P1 {
  const u16 *x, *lar, *aim, *cr, *ci, *ldt, *w1, *w3, *w2, *c, *wm;
  char* ws;
};

constexpr int NWP = 98, NMOD = 28, NKG = 448, NLN = 64, NW2 = 49;

struct cf { float r, i; };
DEV cf cmul(cf a, cf b) { cf o; o.r = a.r * b.r - a.i * b.i; o.i = a.r * b.i + a.i * b.r; return o; }
DEV int qidx(int l) {
  u32 bits = pack2((float)l, 0.f) & 0xffffu;
  return l < 128 ? l : (int)bits - 0x4300 + 128;
}

__global__ __launch_bounds__(256) void k1_prep(P1 p) {
  __shared__ __attribute__((aligned(16))) char smem[4 * 4352 + 1408];
  const int t = threadIdx.x, lane = t & 63, w = t >> 6;
  int b = blockIdx.x;
  if (b < NWP + NMOD) {
    const bool wp = b < NWP;
    const int kb = b / 7, nb = b % 7;
    int colA, colB;
    if (wp) { colA = 3 * D_ + 32 * kb; colB = 4 * D_ + 32 * kb; }
    else { int c0 = 64 * (b - NWP); if (c0 >= 3 * D_) c0 += 2 * D_; colA = c0; colB = c0 + 32; }
    const int rg = t >> 3, pc = t & 7;
    const int col = (pc < 4 ? colA : colB) + 8 * (pc & 3);
    u32 craw = 0;
    if (t < 224) craw = ((const u32*)p.c)[t];
    u32x4 wv[14];
#pragma unroll
    for (int i = 0; i < 14; i++) wv[i] = *(const u32x4*)(p.wm + (size_t)(rg + 32 * i) * (6 * D_) + col);
    const int f8 = t & 31, k8 = (t >> 5) & 3, mat = t >> 7;
    u32x4 ww[8];
    if (wp) {
      const u16* src = (mat ? p.w3 : p.w1) + (size_t)(32 * kb + 8 * k8) * F_ + 256 * nb + 8 * f8;
#pragma unroll
      for (int j = 0; j < 8; j++) ww[j] = *(const u32x4*)(src + (size_t)j * F_);
    }
    float* sc = (float*)smem;
    float* red = sc + 448;
    float* fin = red + 256;
    if (t < 224) {
      float a = lo_f(craw), c2 = hi_f(craw);
      sc[2 * t] = rb(a * __builtin_amdgcn_rcpf(1.f + fexp(-a)));
      sc[2 * t + 1] = rb(c2 * __builtin_amdgcn_rcpf(1.f + fexp(-c2)));
    }
    __syncthreads();
    float acc[8] = {0, 0, 0, 0, 0, 0, 0, 0};
#pragma unroll
    for (int i = 0; i < 14; i++) {
      float s = sc[rg + 32 * i];
#pragma unroll
      for (int q = 0; q < 4; q++) { u32 u = wv[i][q]; acc[2 * q] += s * lo_f(u); acc[2 * q + 1] += s * hi_f(u); }
    }
    {
      bool hi = lane & 32;
      float a4[4];
#pragma unroll
      for (int q = 0; q < 4; q++) { float snd = hi ? acc[q] : acc[q + 4]; float kp = hi ? acc[q + 4] : acc[q]; a4[q] = kp + __shfl_xor(snd, 32); }
      hi = lane & 16;
      float a2[2];
#pragma unroll
      for (int q = 0; q < 2; q++) { float snd = hi ? a4[q] : a4[q + 2]; float kp = hi ? a4[q + 2] : a4[q]; a2[q] = kp + __shfl_xor(snd, 16); }
      hi = lane & 8;
      float snd = hi ? a2[0] : a2[1], kp = hi ? a2[1] : a2[0];
      float a1 = kp + __shfl_xor(snd, 8);
      int e = ((lane >> 5) & 1) * 4 + ((lane >> 4) & 1) * 2 + ((lane >> 3) & 1);
      red[w * 64 + pc * 8 + e] = a1;
    }
    __syncthreads();
    if (t < 64) {
      float v = rb(red[t] + red[64 + t] + red[128 + t] + red[192 + t]);
      if (!wp) ((float*)(p.ws + OFF_MOD))[(t < 32 ? colA : colB - 32) + t] = v;
      else fin[t] = t < 32 ? v : rb(1.f + v);
    }
    if (!wp) return;
    __syncthreads();
    float sb[8], cs[8];
#pragma unroll
    for (int j = 0; j < 8; j++) { sb[j] = fin[8 * k8 + j]; cs[j] = fin[32 + 8 * k8 + j]; }
    const int tau = 16 * nb + (f8 >> 1), cg = tau / 7, tl = tau % 7;
    const int s = 2 * kb + (k8 >> 1);
    u32x4* dst = (u32x4*)(p.ws + OFF_WT) + ((size_t)(cg * 29 + s) * 7 + tl) * 64 + 32 * (k8 & 1) + 16 * mat + 4 * (f8 & 1);
    float ss[8], tt[8];
#pragma unroll
    for (int e = 0; e < 8; e++) {
      float v[8];
#pragma unroll
      for (int j = 0; j < 8; j++) { u32 u = ww[j][e >> 1]; v[j] = (e & 1) ? hi_f(u) : lo_f(u); }
      u32x4 o; float sacc = 0.f, tacc = 0.f;
#pragma unroll
      for (int q = 0; q < 4; q++) {
        u32 pk = pack2(v[2 * q] * cs[2 * q], v[2 * q + 1] * cs[2 * q + 1]);
        o[q] = pk; sacc += lo_f(pk) + hi_f(pk);
        tacc += sb[2 * q] * v[2 * q] + sb[2 * q + 1] * v[2 * q + 1];
      }
      dst[(e & 3) + 8 * (e >> 2)] = o;
      ss[e] = sacc; tt[e] = tacc;
    }
    {
      const bool hi = lane & 32;
      f32x4 o0, o1;
#pragma unroll
      for (int e = 0; e < 8; e++) {
        float snd = hi ? ss[e] : tt[e], kp = hi ? tt[e] : ss[e];
        float r = kp + __shfl_xor(snd, 32);
        if (e < 4) o0[e] = r; else o1[e - 4] = r;
      }
      float* pd = (float*)(p.ws + OFF_PART) + ((size_t)(s * 2 + (hi ? 1 : 0)) * 2 + mat) * F_ + 256 * nb + 8 * f8;
      *(f32x4*)pd = o0; *(f32x4*)(pd + 4) = o1;
    }
    return;
  }
  b -= NWP + NMOD;
  if (b < NKG) {
    const int d = b;
    const float lar = bf2f(p.lar[d * N_ + t]), aim = bf2f(p.aim[d * N_ + t]);
    const float cr = bf2f(p.cr[d * N_ + t]), ci = bf2f(p.ci[d * N_ + t]);
    const float dt = rb(fexp(bf2f(p.ldt[d])));
    const float a2 = -rb(fexp(lar)) * dt * 1.44269504088896341f;
    const float bb = aim * dt;
    const float bh = bb * 0.15915494309189535f;
    const float bl = __builtin_fmaf(bb, 0.15915494309189535f, -bh) + bb * 6.4206382e-9f;
    auto zpow = [&](float pw) {
      float m = __builtin_amdgcn_exp2f(a2 * pw);
      float th = __builtin_amdgcn_fractf(bh * pw) + bl * pw;
      cf o; o.r = m * __builtin_amdgcn_cosf(th); o.i = m * __builtin_amdgcn_sinf(th); return o;
    };
    const cf z1 = zpow(1.f), z8 = zpow(8.f), z128 = zpow(128.f);
    char* buf = smem + w * 4352;
    u32* wr = (u32*)buf + lane;
    const char* rd = buf + (lane & 15) * 272 + (lane >> 4) * 16;
    {
      cf y; y.r = 1.f; y.i = 0.f;
      wr[0] = pack2(1.f, 0.f);
      y = z8; wr[68] = pack2(y.r, -y.i);
#pragma unroll
      for (int c = 2; c < 16; c++) { y = cmul(y, z8); wr[68 * c] = pack2(y.r, -y.i); }
    }
    __builtin_amdgcn_wave_barrier();
    u32x4 bf[4];
#pragma unroll
    for (int s = 0; s < 4; s++) bf[s] = *(const u32x4*)(rd + 64 * s);
    __builtin_amdgcn_wave_barrier();
    f32x4 acc[3];
    cf x[8];
    x[0].r = 2.f * cr * dt; x[0].i = 2.f * ci * dt;
#pragma unroll
    for (int i = 1; i < 8; i++) x[i] = cmul(x[i - 1], z1);
#pragma unroll
    for (int i = 0; i < 8; i++) wr[68 * i] = pack2(x[i].r, x[i].i);
#pragma unroll
    for (int i = 0; i < 8; i++) { x[i] = cmul(x[i], z128); wr[68 * (8 + i)] = pack2(x[i].r, x[i].i); }
    auto tile = [&](int ti) {
      __builtin_amdgcn_wave_barrier();
      f32x4 a = {0, 0, 0, 0};
#pragma unroll
      for (int s = 0; s < 4; s++) a = __builtin_amdgcn_mfma_f32_16x16x32_bf16(asb(*(const u32x4*)(rd + 64 * s)), asb(bf[s]), a, 0, 0, 0);
      acc[ti] = a;
      __builtin_amdgcn_wave_barrier();
    };
    tile(0);
#pragma unroll
    for (int r = 0; r < 2; r++)
#pragma unroll
      for (int i = 0; i < 4; i++) { x[2 * i] = cmul(x[2 * i], z128); wr[68 * (4 * r + i)] = pack2(x[2 * i].r, x[2 * i].i); }
#pragma unroll
    for (int r = 0; r < 4; r++)
#pragma unroll
      for (int i = 0; i < 2; i++) { x[4 * i] = cmul(x[4 * i], z128); wr[68 * (8 + 2 * r + i)] = pack2(x[4 * i].r, x[4 * i].i); }
    tile(1);
#pragma unroll
    for (int r = 0; r < 9; r++) { x[0] = cmul(x[0], z128); wr[68 * r] = pack2(x[0].r, x[0].i); }
    tile(2);
    float* tab = (float*)buf;
    const int c = lane & 15, g = lane >> 4;
#pragma unroll
    for (int r = 0; r < 4; r++) {
      const int row = 4 * g + r;
      tab[(row & 7) + 128 * (row >> 3) + 8 * c] = acc[0][r];
      int q1 = row < 8 ? 256 + 64 * (row >> 2) + (row & 3) + 4 * c : 384 + 32 * ((row - 8) >> 1) + (row & 1) + 2 * c;
      tab[q1] = acc[1][r];
      int q2 = 512 + 16 * row + c;
      if (q2 <= 640) tab[q2] = acc[2][r];
    }
    __syncthreads();
    u16* t16 = (u16*)(smem + 4 * 4352);
#pragma unroll
    for (int i = 0; i < 3; i++) {
      int q = t + 256 * i;
      if (q <= 640) {
        const float* tb = (const float*)smem + q;
        float v = (tb[0] + tb[1088]) + (tb[2176] + tb[3264]);
        t16[q] = (u16)pack2(v, 0.f);
      }
    }
    __syncthreads();
    u32x4 o;
#pragma unroll
    for (int q = 0; q < 4; q++) {
      int l0 = 2047 - 8 * t - 2 * q;
      o[q] = (u32)t16[qidx(l0)] | ((u32)t16[qidx(l0 - 1)] << 16);
    }
    u16* kr = (u16*)(p.ws + OFF_KR) + (size_t)d * KR_STRIDE;
    *(u32x4*)(kr + 8 * t) = o;
    if (t < 2) *(u32x4*)(kr + 2048 + 8 * t) = (u32x4){0, 0, 0, 0};
    return;
  }
  b -= NKG;
  if (b < NLN) {
    const int row0 = 32 * b + 8 * w;
    u32x4 v[8];
    const int ln = lane < 56 ? lane : 55;
#pragma unroll
    for (int i = 0; i < 8; i++) v[i] = *(const u32x4*)(p.x + (size_t)(row0 + i) * D_ + 8 * ln);
    if (lane < 56) {
      u32* xp = (u32*)(p.ws + OFF_XP) + (size_t)(4 * lane) * L_ + row0;
#pragma unroll
      for (int q = 0; q < 4; q++) {
        *(u32x4*)(xp + q * L_) = (u32x4){v[0][q], v[1][q], v[2][q], v[3][q]};
        *(u32x4*)(xp + q * L_ + 4) = (u32x4){v[4][q], v[5][q], v[6][q], v[7][q]};
      }
    }
    const float msk = lane < 56 ? 1.f : 0.f;
    float om = 0.f, ors = 0.f;
#pragma unroll
    for (int i = 0; i < 8; i++) {
      float s = 0.f;
#pragma unroll
      for (int q = 0; q < 4; q++) { u32 u = v[i][q]; s += lo_f(u) + hi_f(u); }
      float m = wave_sum(s * msk) * (1.f / D_);
      float qq = 0.f;
#pragma unroll
      for (int q = 0; q < 4; q++) { u32 u = v[i][q]; float a = lo_f(u) - m, c2 = hi_f(u) - m; qq += a * a + c2 * c2; }
      float var = wave_sum(qq * msk) * (1.f / D_);
      if (lane == i) { om = m; ors = var; }
    }
    if (lane < 8) {
      float sd = rb(__builtin_sqrtf(rb(rb(ors) + 1e-5f)));
      ((f32x2*)(p.ws + OFF_ST1))[row0 + lane] = (f32x2){rb(om), 1.f / sd};
    }
    return;
  }
  b -= NLN;
  {
    const int g = b * 256 + t;
    const int d8 = g % 56, f8 = g / 56;
    u32x4 ww[8];
#pragma unroll
    for (int j = 0; j < 8; j++) ww[j] = *(const u32x4*)(p.w2 + (size_t)(8 * f8 + j) * D_ + 8 * d8);
    const int dt = d8 >> 2, hh = (d8 >> 1) & 1, i8 = d8 & 1;
    u32x4* dst = (u32x4*)(p.ws + OFF_W2T) + ((size_t)dt * 112 + (f8 >> 1)) * 64 + 32 * (f8 & 1) + 4 * hh + 16 * i8;
#pragma unroll
    for (int e = 0; e < 8; e++) {
      u32x4 o;
#pragma unroll
      for (int q = 0; q < 4; q++) {
        u32 a = ww[2 * q][e >> 1], c2 = ww[2 * q + 1][e >> 1];
        o[q] = (e & 1) ? ((a >> 16) | (c2 & 0xffff0000u)) : ((a & 0xffffu) | (c2 << 16));
      }
      dst[(e & 3) + 8 * (e >> 2)] = o;
    }
  }
}

struct P2 { const u16* dsk; char* ws; };
constexpr int KC_BYTES = 4 * 4160;
constexpr int HP_BYTES = 4 * 80 * 16;
constexpr int CH_BYTES = KC_BYTES + HP_BYTES;

__global__ __launch_bounds__(256) void k2_conv(P2 p) {
  __shared__ __attribute__((aligned(16))) char smem[2 * CH_BYTES + 2 * 4096];
  const int t = threadIdx.x, lane = t & 63, w = t >> 6;
  const int b = blockIdx.x;
  if (b >= 224) {
    const int g = (b - 224) * 256 + t;
    const float* ps = (const float*)(p.ws + OFF_PART) + g;
    float s = 0.f, tt = 0.f;
#pragma unroll
    for (int k = 0; k < 28; k++) { s += ps[(size_t)(2 * k) * 3584]; tt += ps[(size_t)(2 * k + 1) * 3584]; }
    const int mat = g / F_, f = g % F_;
    const int tau = f >> 4, fl = f & 15, cg = tau / 7, tl = tau % 7;
    const int rho = 16 * mat + ((fl & 3) | ((fl & 4) << 1) | ((fl & 8) >> 1));
    float s0 = rb(s), s1 = rb(s - s0), s2 = rb(s - s0 - s1);
    float t0 = rb(tt), t1 = rb(tt - t0), t2 = rb(tt - t0 - t1);
    u32x4* dst = (u32x4*)(p.ws + OFF_WT) + ((size_t)(cg * 29 + 28) * 7 + tl) * 64 + rho;
    dst[0] = (u32x4){pack2(s0, s1), pack2(s2, t0), pack2(t1, t2), 0u};
    dst[32] = (u32x4){0, 0, 0, 0};
    return;
  }
  const int d0 = 2 * b;
  const u32x4* xp = (const u32x4*)(p.ws + OFF_XP + (size_t)b * L_ * 4) + 2 * t;
  u32x4 xa = xp[0], xb = xp[1];
  const f32x4* stp = (const f32x4*)(p.ws + OFF_ST1) + 4 * t;
  f32x4 st[4];
#pragma unroll
  for (int i = 0; i < 4; i++) st[i] = stp[i];
  u32x4 ka[2]; u32x2 kb[2];
#pragma unroll
  for (int c = 0; c < 2; c++) {
    const u16* kr = (const u16*)(p.ws + OFF_KR) + (size_t)(d0 + c) * KR_STRIDE + 8 * t;
    ka[c] = *(const u32x4*)kr; kb[c] = *(const u32x2*)(kr + 8);
  }
  const float* mod = (const float*)(p.ws + OFF_MOD);
  const f32x2 sa = *(const f32x2*)(mod + d0), ca = *(const f32x2*)(mod + D_ + d0), ga = *(const f32x2*)(mod + 2 * D_ + d0);
  const u32 dskr = *(const u32*)(p.dsk + d0);
  if (t < 128) {
    const int c = t >> 6, pl = (t >> 4) & 3, i = t & 15;
    *(u32x4*)(smem + c * CH_BYTES + KC_BYTES + (pl * 80 + i) * 16) = (u32x4){0, 0, 0, 0};
  } else if (t < 160) {
    const int u = t - 128, c = u >> 4, o = (u >> 2) & 3, i = u & 3;
    *(u32x4*)(smem + c * CH_BYTES + o * 4160 + 4096 + i * 16) = (u32x4){0, 0, 0, 0};
  }
#pragma unroll
  for (int c = 0; c < 2; c++) {
    u32 wv[6] = {ka[c][0], ka[c][1], ka[c][2], ka[c][3], kb[c][0], kb[c][1]};
    u32 al[5];
#pragma unroll
    for (int i = 0; i < 5; i++) al[i] = __builtin_amdgcn_alignbit(wv[i + 1], wv[i], 16);
    char* base = smem + c * CH_BYTES + 16 * t;
    *(u32x4*)(base) = (u32x4){wv[0], wv[1], wv[2], wv[3]};
    *(u32x4*)(base + 4160) = (u32x4){al[0], al[1], al[2], al[3]};
    *(u32x4*)(base + 2 * 4160) = (u32x4){wv[1], wv[2], wv[3], wv[4]};
    *(u32x4*)(base + 3 * 4160) = (u32x4){al[1], al[2], al[3], al[4]};
  }
  u32x4 hp[2];
  {
    const float c1a = rb(1.f + ca[0]), c1b = rb(1.f + ca[1]);
    float h0[8], h1[8];
#pragma unroll
    for (int i = 0; i < 8; i++) {
      u32 u = i < 4 ? xa[i] : xb[i - 4];
      float m = st[i >> 1][2 * (i & 1)], rs = st[i >> 1][2 * (i & 1) + 1];
      u32 dm = pack2(lo_f(u) - m, hi_f(u) - m);
      u32 n = pack2(lo_f(dm) * rs, hi_f(dm) * rs);
      u32 sc2 = pack2(lo_f(n) * c1a, hi_f(n) * c1b);
      h0[i] = lo_f(sc2) + sa[0]; h1[i] = hi_f(sc2) + sa[1];
    }
#pragma unroll
    for (int q = 0; q < 4; q++) { hp[0][q] = pack2(h0[2 * q], h0[2 * q + 1]); hp[1][q] = pack2(h1[2 * q], h1[2 * q + 1]); }
#pragma unroll
    for (int c = 0; c < 2; c++) *(u32x4*)(smem + c * CH_BYTES + KC_BYTES + ((t & 3) * 80 + 16 + (t >> 2)) * 16) = hp[c];
  }
  __syncthreads();
  const int ch = w >> 1, mt = w & 1, r = lane & 15, g = lane >> 4;
  const char* abase = smem + ch * CH_BYTES + (3 - (r & 3)) * 4160 + 2 * (2044 - 16 * mt - 4 * (r >> 2) + 8 * g);
  const char* bbase = smem + ch * CH_BYTES + KC_BYTES + (g * 80 + 16 + r) * 16;
  f32x4 acc[4] = {};
#pragma unroll
  for (int k0 = 0; k0 < 16; k0++) {
    u32x4 A[4], B[4];
#pragma unroll
    for (int a = 0; a < 4; a++) {
      const char* ap = abase - 1024 * a - 64 * k0;
      u32x2 lo = *(const u32x2*)ap, hi = *(const u32x2*)(ap + 8);
      A[a] = (u32x4){lo[0], lo[1], hi[0], hi[1]};
      B[a] = *(const u32x4*)(bbase + 256 * a - 16 * k0);
    }
#pragma unroll
    for (int a = 0; a < 4; a++)
#pragma unroll
      for (int nt = a; nt < 4; nt++)
        acc[nt] = __builtin_amdgcn_mfma_f32_16x16x32_bf16(asb(A[a]), asb(B[nt - a]), acc[nt], 0, 0, 0);
  }
  char* ybuf = smem + 2 * CH_BYTES;
#pragma unroll
  for (int nt = 0; nt < 4; nt++)
    *(u32x2*)(ybuf + ch * 4096 + 2 * (32 * (16 * nt + r) + 16 * mt + 4 * g)) = (u32x2){pack2(acc[nt][0], acc[nt][1]), pack2(acc[nt][2], acc[nt][3])};
  __syncthreads();
#pragma unroll
  for (int c = 0; c < 2; c++) {
    u32x4 y = *(const u32x4*)(ybuf + c * 4096 + 16 * t);
    const float dk = c ? hi_f(dskr) : lo_f(dskr), gg = ga[c];
    u32x4 o;
#pragma unroll
    for (int q = 0; q < 4; q++) {
      u32 hq = hp[c][q], yq = y[q];
      u32 hd = pack2(lo_f(hq) * dk, hi_f(hq) * dk);
      u32 a = pack2(lo_f(yq) + lo_f(hd), hi_f(yq) + hi_f(hd));
      u32 m = pack2(lo_f(a) * gg, hi_f(a) * gg);
      u32 x0 = q < 2 ? xa[2 * q] : xb[2 * q - 4], x1 = q < 2 ? xa[2 * q + 1] : xb[2 * q - 3];
      float xv0 = c ? hi_f(x0) : lo_f(x0), xv1 = c ? hi_f(x1) : lo_f(x1);
      o[q] = pack2(xv0 + lo_f(m), xv1 + hi_f(m));
    }
    *(u32x4*)(p.ws + OFF_X1T + (size_t)(d0 + c) * L_ * 2 + 16 * t) = o;
  }
}

struct P3 { char* ws; };
constexpr int SLOT = 22 * 1024, NSLOT = 6, EXT_OFF = NSLOT * SLOT;
constexpr int K3_LDS = EXT_OFF + 7 * 1024;

DEV void glds16(const void* gsrc, u32 lds_dst) {
  u32 keep;
  asm volatile("s_mov_b32 %0, m0\n\ts_mov_b32 m0, %2\n\ts_nop 0\n\tglobal_load_lds_dwordx4 %1, off\n\ts_mov_b32 m0, %0"
               : "=&s"(keep) : "v"(gsrc), "s"(lds_dst) : "memory");
}
template <int N> DEV void wait_vm() { asm volatile("s_waitcnt vmcnt(%0)" :: "i"(N) : "memory"); }
DEV void bar() { asm volatile("s_waitcnt lgkmcnt(0)\n\ts_barrier" ::: "memory"); }

template <int NT>
DEV void k3_body(char* ws, char* smem, int w, int lane, int cg, int pm) {
  const int mt = w & 3, nh = w >> 2, tl0 = nh * 4;
  const u32 lbase = (u32)(size_t)smem;
  const char* wsrc = ws + OFF_WT + (size_t)cg * 29 * 7168 + lane * 16;
  const char* xsrc = ws + OFF_X1T + (size_t)(4 * w + ((lane >> 2) & 3)) * (L_ * 2) + (pm * 128 + 32 * (lane >> 4) + 8 * (lane & 3)) * 2;
  const bool three = w < 6;
  auto issue = [&](int c) {
    const u32 sl = lbase + (c % NSLOT) * SLOT;
    glds16(xsrc + (size_t)c * 32 * (L_ * 2), sl + 14336 + w * 1024);
    glds16(wsrc + c * 14336 + w * 1024, sl + w * 1024);
    if (three) glds16(wsrc + c * 14336 + (8 + w) * 1024, sl + (8 + w) * 1024);
  };
  if (w < 7) glds16(wsrc + 28 * 7168 + w * 1024, lbase + EXT_OFF + w * 1024);
#pragma unroll
  for (int c = 0; c < NSLOT; c++) issue(c);

  const char* wrd = smem + tl0 * 1024 + lane * 16;
  const char* xrd = smem + 14336 + (lane >> 5) * 2048 + mt * 256 + ((lane & 15) >> 2) * 64 + ((lane >> 4) & 1) * 32 + (lane & 3) * 8;
  f32x16 acc[NT];
#pragma unroll
  for (int i = 0; i < NT; i++) acc[i] = (f32x16){};
  float s1 = 0.f, s2 = 0.f;
  u32x4 Wf[2][NT], Xf[2];
  auto rd = [&](int c, int ks) {
    const int so = (c % NSLOT) * SLOT;
    s16x4 a = __builtin_amdgcn_ds_read_tr16_b64_v4i16((LAS s16x4*)(xrd + so + ks * 4096));
    s16x4 b2 = __builtin_amdgcn_ds_read_tr16_b64_v4i16((LAS s16x4*)(xrd + so + ks * 4096 + 1024));
    u32x2 a2 = __builtin_bit_cast(u32x2, a), b3 = __builtin_bit_cast(u32x2, b2);
    Xf[ks] = (u32x4){a2[0], a2[1], b3[0], b3[1]};
#pragma unroll
    for (int i = 0; i < NT; i++) Wf[ks][i] = *(const u32x4*)(wrd + so + ks * 7168 + i * 1024);
  };
  auto mm = [&](int ks) {
#pragma unroll
    for (int i = 0; i < NT; i++) acc[i] = __builtin_amdgcn_mfma_f32_32x32x16_bf16(asb(Wf[ks][i]), asb(Xf[ks]), acc[i], 0, 0, 0);
#pragma unroll
    for (int q = 0; q < 4; q++) { u32 u = Xf[ks][q]; s1 = dot2(u, 0x3f803f80u, s1); s2 = dot2(u, u, s2); }
  };
#define K3_STEP(c)                                                                       \
  {                                                                                      \
    constexpr int last = (c) + 4 < 5 ? 5 : ((c) + 4 > 13 ? 13 : (c) + 4);                \
    if (three) wait_vm<3 * (last - (c))>(); else wait_vm<2 * (last - (c))>();            \
    bar();                                                                               \
    rd(c, 0);                                                                            \
    __builtin_amdgcn_sched_barrier(0);                                                   \
    if ((c) >= 1 && (c) + 5 <= 13) issue((c) + 5);                                       \
    if ((c) >= 1) mm(1);                                                                 \
    __builtin_amdgcn_sched_barrier(0);                                                   \
    rd(c, 1);                                                                            \
    __builtin_amdgcn_sched_barrier(0);                                                   \
    mm(0);                                                                               \
    __builtin_amdgcn_sched_barrier(0);                                                   \
  }
  K3_STEP(0) K3_STEP(1) K3_STEP(2) K3_STEP(3) K3_STEP(4) K3_STEP(5) K3_STEP(6)
  K3_STEP(7) K3_STEP(8) K3_STEP(9) K3_STEP(10) K3_STEP(11) K3_STEP(12) K3_STEP(13)
#undef K3_STEP
#pragma unroll
  for (int i = 0; i < NT; i++) Wf[0][i] = *(const u32x4*)(smem + EXT_OFF + (tl0 + i) * 1024 + lane * 16);
  mm(1);
  s1 += __shfl_xor(s1, 32); s2 += __shfl_xor(s2, 32);
  const float mean = s1 * (1.f / D_);
  const float var = fmaxf(s2 * (1.f / D_) - mean * mean, 0.f);
  const float sd = rb(__builtin_sqrtf(rb(rb(var) + 1e-5f)));
  const float rs = 1.f / sd;
  u32x4 ex = (u32x4){pack2(-mean, -mean), pack2(-mean, sd), pack2(sd, sd), 0u};
  if (lane >= 32) ex = (u32x4){0, 0, 0, 0};
#pragma unroll
  for (int i = 0; i < NT; i++) acc[i] = __builtin_amdgcn_mfma_f32_32x32x16_bf16(asb(Wf[0][i]), asb(ex), acc[i], 0, 0, 0);
  const float c1 = -rs * 1.44269504088896341f, c2 = rs * rs;
  u32x4* hd = (u32x4*)(ws + OFF_H) + ((size_t)(pm * 4 + mt) * 112 + cg * 7 + tl0) * 64 + lane;
#pragma unroll
  for (int i = 0; i < NT; i++) {
    float hv[8];
#pragma unroll
    for (int j = 0; j < 8; j++) {
      float gg = acc[i][j], uu = acc[i][8 + j];
      float e = __builtin_amdgcn_exp2f(gg * c1);
      hv[j] = (gg * uu * c2) * __builtin_amdgcn_rcpf(1.f + e);
    }
    hd[i * 64] = (u32x4){pack2(hv[0], hv[1]), pack2(hv[2], hv[3]), pack2(hv[4], hv[5]), pack2(hv[6], hv[7])};
  }
}

__global__ __launch_bounds__(512, 2) void k3_gateup(P3 p) {
  __shared__ __attribute__((aligned(16))) char smem[K3_LDS];
  const int lane = threadIdx.x & 63;
  const int w = __builtin_amdgcn_readfirstlane(threadIdx.x >> 6);
  const int b = blockIdx.x, xcd = b & 7, i = b >> 3;
  const int pm = (xcd & 1) * 8 + (i & 7), cg = (xcd >> 1) * 4 + (i >> 3);
  if (w < 4) k3_body<4>(p.ws, smem, w, lane, cg, pm);
  else k3_body<3>(p.ws, smem, w, lane, cg, pm);
}

struct P4 { char* ws; u16* out; };
constexpr int PF = 10;

__global__ __launch_bounds__(256, 1) void k4_down(P4 p) {
  __shared__ __attribute__((aligned(16))) char smem[4 * 3 * 4096];
  const int lane = threadIdx.x & 63;
  const int w = __builtin_amdgcn_readfirstlane(threadIdx.x >> 6);
  const int b = blockIdx.x, xcd = b & 7, i = b >> 3;
  const int mg = xcd * 4 + (i & 3), dg = i >> 2;
  const u32x4* ha = (const u32x4*)(p.ws + OFF_H) + ((size_t)(2 * mg) * 112 + 28 * w) * 64 + lane;
  const u32x4* wa = (const u32x4*)(p.ws + OFF_W2T) + ((size_t)(2 * dg) * 112 + 28 * w) * 64 + lane;
  const int odt = w >> 1, omt = w & 1;
  const int orow = 64 * mg + 32 * omt + (lane & 31), ocol = 64 * dg + 32 * odt + 16 * (lane >> 5);
  u32x4 R[PF][4];
#pragma unroll
  for (int s = 0; s < PF; s++) {
    R[s][0] = wa[s * 64]; R[s][1] = wa[(112 + s) * 64]; R[s][2] = ha[s * 64]; R[s][3] = ha[(112 + s) * 64];
  }
  u16 xr[16];
  const u16* x1 = (const u16*)(p.ws + OFF_X1T) + (size_t)ocol * L_ + orow;
  f32x4 gbv[4];
  f32x16 acc[4];
#pragma unroll
  for (int q = 0; q < 4; q++) acc[q] = (f32x16){};
#pragma unroll
  for (int s = 0; s < 28; s++) {
    const int sl = s % PF;
    u32x4 a0 = R[sl][0], a1 = R[sl][1], b0 = R[sl][2], b1 = R[sl][3];
    acc[0] = __builtin_amdgcn_mfma_f32_32x32x16_bf16(asb(a0), asb(b0), acc[0], 0, 0, 0);
    acc[1] = __builtin_amdgcn_mfma_f32_32x32x16_bf16(asb(a0), asb(b1), acc[1], 0, 0, 0);
    acc[2] = __builtin_amdgcn_mfma_f32_32x32x16_bf16(asb(a1), asb(b0), acc[2], 0, 0, 0);
    acc[3] = __builtin_amdgcn_mfma_f32_32x32x16_bf16(asb(a1), asb(b1), acc[3], 0, 0, 0);
    if (s + PF < 28) {
      const int n = s + PF;
      R[sl][0] = wa[n * 64]; R[sl][1] = wa[(112 + n) * 64]; R[sl][2] = ha[n * 64]; R[sl][3] = ha[(112 + n) * 64];
    }
    if (s == 28 - PF) {
#pragma unroll
      for (int j = 0; j < 16; j++) xr[j] = x1[(size_t)j * L_];
#pragma unroll
      for (int j = 0; j < 4; j++) gbv[j] = *(const f32x4*)((const float*)(p.ws + OFF_MOD) + 5 * D_ + ocol + 4 * j);
    }
    __builtin_amdgcn_sched_barrier(0);
  }
#pragma unroll
  for (int q = 0; q < 4; q++) {
    if (q != w) {
      const int slot = w < q ? w : w - 1;
      f32x4* dst = (f32x4*)(smem + (q * 3 + slot) * 4096) + lane;
#pragma unroll
      for (int j = 0; j < 4; j++) dst[j * 64] = (f32x4){acc[q][4 * j], acc[q][4 * j + 1], acc[q][4 * j + 2], acc[q][4 * j + 3]};
    }
  }
  __syncthreads();
  f32x16 o = w == 0 ? acc[0] : (w == 1 ? acc[1] : (w == 2 ? acc[2] : acc[3]));
#pragma unroll
  for (int sl = 0; sl < 3; sl++) {
    const f32x4* src = (const f32x4*)(smem + (w * 3 + sl) * 4096) + lane;
#pragma unroll
    for (int j = 0; j < 4; j++) { f32x4 v = src[j * 64]; o[4 * j] += v[0]; o[4 * j + 1] += v[1]; o[4 * j + 2] += v[2]; o[4 * j + 3] += v[3]; }
  }
  u32 res[8];
#pragma unroll
  for (int j = 0; j < 8; j++) {
    u32 f = pack2(o[2 * j], o[2 * j + 1]);
    u32 m = pack2(lo_f(f) * gbv[j >> 1][(2 * j) & 3], hi_f(f) * gbv[j >> 1][(2 * j + 1) & 3]);
    res[j] = pack2(bf2f(xr[2 * j]) + lo_f(m), bf2f(xr[2 * j + 1]) + hi_f(m));
  }
  u32x4* op = (u32x4*)(p.out + (size_t)orow * D_ + ocol);
  op[0] = (u32x4){res[0], res[1], res[2], res[3]};
  op[1] = (u32x4){res[4], res[5], res[6], res[7]};
}

extern "C" void kernel_launch(void* const* d_in, const int* in_sizes, int n_in,
                              void* d_out, int out_size, void* d_ws, size_t ws_size,
                              hipStream_t stream) {
  P1 p1;
  p1.x = (const u16*)d_in[0]; p1.lar = (const u16*)d_in[1]; p1.aim = (const u16*)d_in[2];
  p1.cr = (const u16*)d_in[3]; p1.ci = (const u16*)d_in[4]; p1.ldt = (const u16*)d_in[5];
  p1.w1 = (const u16*)d_in[8]; p1.w3 = (const u16*)d_in[9]; p1.w2 = (const u16*)d_in[10];
  p1.c = (const u16*)d_in[11]; p1.wm = (const u16*)d_in[12]; p1.ws = (char*)d_ws;
  k1_prep<<<NWP + NMOD + NKG + NLN + NW2, 256, 0, stream>>>(p1);
  P2 p2; p2.dsk = (const u16*)d_in[6]; p2.ws = (char*)d_ws;
  k2_conv<<<224 + 14, 256, 0, stream>>>(p2);
  P3 p3; p3.ws = (char*)d_ws;
  k3_gateup<<<256, 512, 0, stream>>>(p3);
  P4 p4; p4.ws = (char*)d_ws; p4.out = (u16*)d_out;
  k4_down<<<224, 256, 0, stream>>>(p4);
}
```

```cpp
#include <hip/hip_runtime.h>
#include <stdint.h>
#include <type_traits>

typedef unsigned short u16;
typedef unsigned int u32;
typedef __attribute__((ext_vector_type(8))) __bf16 bf16x8;
typedef __attribute__((ext_vector_type(2))) __bf16 bf16x2;
typedef __attribute__((ext_vector_type(8))) _Float16 f16x8;
typedef __attribute__((ext_vector_type(2))) _Float16 f16x2;
typedef __attribute__((ext_vector_type(4))) short s16x4;
typedef __attribute__((ext_vector_type(2))) float f32x2;
typedef __attribute__((ext_vector_type(4))) float f32x4;
typedef __attribute__((ext_vector_type(16))) float f32x16;
typedef __attribute__((ext_vector_type(4))) u32 u32x4;
typedef __attribute__((ext_vector_type(2))) u32 u32x2;
#define LAS __attribute__((address_space(3)))
#define DEV __device__ __forceinline__

constexpr int L_ = 2048, D_ = 448, N_ = 256, F_ = 1792;
constexpr u32 OFF_E    = 0;
constexpr u32 OFF_CTR  = 4096;
constexpr u32 OFF_W2F  = OFF_CTR + 160 * 128;
constexpr u32 OFF_STG  = 32768;
constexpr u32 OFF_MODG = 49152;
constexpr u32 OFF_ST2G = 65536;
constexpr u32 OFF_CSG  = 81920;
constexpr u32 OFF_XG   = 147456;
constexpr u32 OFF_X1T  = OFF_XG + 224 * L_ * 8;
constexpr u32 OFF_X1S  = OFF_X1T + D_ * L_ * 2;
constexpr u32 OFF_W2T  = OFF_X1S + D_ * L_ * 2;
constexpr u32 OFF_H    = OFF_W2T + 14 * 112 * 1024;
constexpr size_t OFF_DBG = (size_t)64 << 20;
constexpr int SPIN_CAP = 1 << 14;

constexpr int R1 = 13, R2 = 10, R3 = 5;
constexpr int KGW = 4352;
constexpr int LDS_KG  = 0;
constexpr int LDS_T16 = 8 * KGW;
constexpr int LDS_TX  = LDS_T16 + 2816;
constexpr int LDS_S1  = 45056;
constexpr int LDS_CNT = 163840 - 64;
constexpr int LDS_TOTAL = 163840;
constexpr int KC_BYTES = 4 * 4160, HP_BYTES = 2 * 144 * 16, CH_BYTES = KC_BYTES + HP_BYTES;
constexpr int LDS_CV  = R2 * 8192;
constexpr int LDS_Y   = LDS_CV + 2 * CH_BYTES;
constexpr int LDS_R3  = 114688;
static_assert(LDS_TX + 8 * 912 <= LDS_S1 && LDS_S1 + R1 * 8192 <= LDS_CNT && LDS_Y + 32768 <= LDS_CNT && LDS_R3 + R3 * 8192 <= LDS_CNT && R1 + R2 + R3 == 28, "lds map");

#define STAMP(i) do {} while (0)
#define STAMP_DECL do {} while (0)
#define STAMP_OUT do {} while (0)

DEV u32 pack2(float lo, float hi) { f32x2 v = {lo, hi}; bf16x2 b = __builtin_convertvector(v, bf16x2); return __builtin_bit_cast(u32, b); }
DEV u32 packh(float lo, float hi) { f32x2 v = {lo, hi}; f16x2 b = __builtin_convertvector(v, f16x2); return __builtin_bit_cast(u32, b); }
DEV float lo_f(u32 u) { return __uint_as_float(u << 16); }
DEV float hi_f(u32 u) { return __uint_as_float(u & 0xffff0000u); }
DEV float rb(float f) { return lo_f(pack2(f, f)); }
DEV float dot2(u32 a, u32 b, float c) { return __builtin_amdgcn_fdot2_f32_bf16(__builtin_bit_cast(bf16x2, a), __builtin_bit_cast(bf16x2, b), c, false); }
DEV bf16x8 asb(u32x4 v) { return __builtin_bit_cast(bf16x8, v); }
DEV f16x8 ash(u32x4 v) { return __builtin_bit_cast(f16x8, v); }
DEV float fexp(float x) { return __builtin_amdgcn_exp2f(x * 1.44269504088896341f); }
DEV u32 rfl(u32 v) { return __builtin_amdgcn_readfirstlane(v); }

template <int CTRL, int RMASK> DEV float dppf(float v) { return __int_as_float(__builtin_amdgcn_update_dpp(0, __float_as_int(v), CTRL, RMASK, 0xf, false)); }
DEV float wave_sum(float v) {
  v += dppf<0x111, 0xf>(v); v += dppf<0x112, 0xf>(v); v += dppf<0x114, 0xf>(v); v += dppf<0x118, 0xf>(v);
  v += dppf<0x142, 0xa>(v); v += dppf<0x143, 0xc>(v);
  return __int_as_float(__builtin_amdgcn_readlane(__float_as_int(v), 63));
}
DEV float wave_max(float v) {
  v = fmaxf(v, dppf<0x111, 0xf>(v)); v = fmaxf(v, dppf<0x112, 0xf>(v)); v = fmaxf(v, dppf<0x114, 0xf>(v)); v = fmaxf(v, dppf<0x118, 0xf>(v));
  v = fmaxf(v, dppf<0x142, 0xa>(v)); v = fmaxf(v, dppf<0x143, 0xc>(v));
  return __int_as_float(__builtin_amdgcn_readlane(__float_as_int(v), 63));
}
DEV float red32(float a, float b) { auto r = __builtin_amdgcn_permlane32_swap(__float_as_uint(a), __float_as_uint(b), false, false); return __uint_as_float(r[0]) + __uint_as_float(r[1]); }
DEV float red16(float a, float b) { auto r = __builtin_amdgcn_permlane16_swap(__float_as_uint(a), __float_as_uint(b), false, false); return __uint_as_float(r[0]) + __uint_as_float(r[1]); }

#define LD16(d, off, base)    asm volatile("global_load_dwordx4 %0, %1, %2" : "=v"(d) : "v"(off), "s"(base) : "memory")
#define LD16S(d, off, base)   asm volatile("global_load_dwordx4 %0, %1, %2 sc1" : "=v"(d) : "v"(off), "s"(base) : "memory")
#define LD8S(d, off, base)    asm volatile("global_load_dwordx2 %0, %1, %2 sc1" : "=v"(d) : "v"(off), "s"(base) : "memory")
#define LD4(d, off, base)     asm volatile("global_load_dword %0, %1, %2" : "=v"(d) : "v"(off), "s"(base) : "memory")
#define LD4S(d, off, base)    asm volatile("global_load_dword %0, %1, %2 sc1" : "=v"(d) : "v"(off), "s"(base) : "memory")
#define LD2(d, off, base)     asm volatile("global_load_ushort %0, %1, %2" : "=v"(d) : "v"(off), "s"(base) : "memory")
#define LD2S(d, off, base)    asm volatile("global_load_ushort %0, %1, %2 sc1" : "=v"(d) : "v"(off), "s"(base) : "memory")
#define ST16S(off, v, base)   asm volatile("global_store_dwordx4 %0, %1, %2 sc1\n\ts_nop 1" :: "v"(off), "v"(v), "s"(base) : "memory")
#define ST8S(off, v, base)    asm volatile("global_store_dwordx2 %0, %1, %2 sc1" :: "v"(off), "v"(v), "s"(base) : "memory")
#define ST4S(off, v, base)    asm volatile("global_store_dword %0, %1, %2 sc1" :: "v"(off), "v"(v), "s"(base) : "memory")
#define ATOM_INC(off, base)   asm volatile("global_atomic_add %0, %1, %2" :: "v"(off), "v"(1u), "s"(base) : "memory")
#define DMA(off, base, lds)   asm volatile("s_mov_b32 m0, %2\n\ts_nop 0\n\tglobal_load_lds_dwordx4 %0, %1" :: "v"(off), "s"(base), "s"(lds) : "memory")
#define DMAS(off, base, lds)  asm volatile("s_mov_b32 m0, %2\n\ts_nop 0\n\tglobal_load_lds_dwordx4 %0, %1 sc1" :: "v"(off), "s"(base), "s"(lds) : "memory")
#define WAITV(n)              asm volatile("s_waitcnt vmcnt(%0)" :: "i"(n) : "memory")
#define PIN(x)                asm volatile("" : "+v"(x))
#define BAR()                 asm volatile("s_waitcnt lgkmcnt(0)\n\ts_barrier" ::: "memory")
#define BAR_RAW()             asm volatile("s_barrier" ::: "memory")
#define SCHED()               __builtin_amdgcn_sched_barrier(0)

DEV u32x4 tr_frag(const char* p0, const char* p1) {
  u32x2 a = __builtin_bit_cast(u32x2, __builtin_amdgcn_ds_read_tr16_b64_v4i16((LAS s16x4*)p0));
  u32x2 b = __builtin_bit_cast(u32x2, __builtin_amdgcn_ds_read_tr16_b64_v4i16((LAS s16x4*)p1));
  return (u32x4){a[0], a[1], b[0], b[1]};
}

struct Params { const u16 *x, *lar, *aim, *cr, *ci, *ldt, *dsk, *w1, *w3, *w2, *c, *wm; char* ws; u16* out; };
struct cm { f32x2 b, bp; };
DEV cm mk_cm(float r, float i) { cm m; m.b = (f32x2){r, i}; m.bp = (f32x2){-i, r}; return m; }
DEV f32x2 cmul(f32x2 a, cm m) { f32x2 ax = {a[0], a[0]}, ay = {a[1], a[1]}; return __builtin_elementwise_fma(ay, m.bp, ax * m.b); }
DEV u32 stat_payload(float s1, float s2) {
  const float m = s1 * (1.f / D_);
  const float var = fmaxf(s2 * (1.f / D_) - m * m, 0.f);
  return pack2(m, __builtin_amdgcn_sqrtf(rb(rb(var) + 1e-5f)));
}
DEV void poll_eq14(char* ws, u32 off) {
#pragma nounroll
  for (int sp = 0; sp < SPIN_CAP; sp++) {
    u32 v; asm volatile("global_load_dword %0, %1, %2 sc1\n\ts_waitcnt vmcnt(0)" : "=&v"(v) : "v"(off), "s"(ws) : "memory");
    if (__all(v == 14u)) break;
  }
}
DEV f32x2 up2(u32 u) { return (f32x2){lo_f(u), hi_f(u)}; }
DEV u32 pk2(f32x2 v) { return pack2(v[0], v[1]); }
#define DMA64(addr, lds)  asm volatile("s_mov_b32 m0, %1\n\ts_nop 0\n\tglobal_load_lds_dwordx4 %0, off" :: "v"(addr), "s"(lds) : "memory")
constexpr int rpos(int r) { return (r < 8 ? r : (r ^ 4)) * 272; }
template <int R0> DEV void wrt8(u32 m0v, const u32 (&v)[8]) {
  asm volatile("s_mov_b32 m0, %8\n\ts_nop 0\n\t"
               "ds_write_addtid_b32 %0 offset:%9\n\tds_write_addtid_b32 %1 offset:%10\n\tds_write_addtid_b32 %2 offset:%11\n\tds_write_addtid_b32 %3 offset:%12\n\t"
               "ds_write_addtid_b32 %4 offset:%13\n\tds_write_addtid_b32 %5 offset:%14\n\tds_write_addtid_b32 %6 offset:%15\n\tds_write_addtid_b32 %7 offset:%16"
               :: "v"(v[0]), "v"(v[1]), "v"(v[2]), "v"(v[3]), "v"(v[4]), "v"(v[5]), "v"(v[6]), "v"(v[7]), "s"(m0v),
                  "i"(rpos(R0)), "i"(rpos(R0 + 1)), "i"(rpos(R0 + 2)), "i"(rpos(R0 + 3)), "i"(rpos(R0 + 4)), "i"(rpos(R0 + 5)), "i"(rpos(R0 + 6)), "i"(rpos(R0 + 7)) : "memory");
}
DEV u32 pkh(f32x2 v) { return packh(v[0], v[1]); }
DEV int qidx(int l) {
  u32 bits = pack2((float)l, 0.f) & 0xffffu;
  int q = l < 128 ? l : (int)bits - 0x4300 + 128;
  return l < 0 ? 641 : q;
}

__global__ __launch_bounds__(512, 2) void fused_kernel(Params p) {
  __shared__ __attribute__((aligned(16))) char smem[LDS_TOTAL];
  const int t = threadIdx.x, lane = t & 63;
  const int w = rfl(t >> 6);
  const int b = blockIdx.x, xcd = b & 7, bi = b >> 3;
  const bool is_main = bi < 28;
  char* const ws = p.ws;
  const u32 lbase = (u32)(size_t)smem;
  STAMP_DECL;
  STAMP(0);

  if (t == 0 && b < 160) { u32 o = OFF_CTR + b * 128; ST4S(o, 0u, ws); }
  if (t < 2) *(u32*)(smem + LDS_CNT + 4 * t) = 0;
  const int row = 8 * b + w;
#define LN_LOAD() u32x4 xr; u32 ep; { u32 o = (u32)row * 896 + (lane < 56 ? lane : 55) * 16; LD16(xr, o, p.x); u32 z = 0; LD4S(ep, z, ws); }

  auto ln_publish = [&](u32x4 xr, u32 tag) {
    float s1 = 0.f, s2 = 0.f;
    if (lane < 56) {
      *(u32x4*)(smem + LDS_TX + w * 912 + lane * 16) = xr;
      float a1 = 0.f, a2 = 0.f;
      s1 = dot2(xr[0], 0x3f803f80u, s1); s2 = dot2(xr[0], xr[0], s2); a1 = dot2(xr[1], 0x3f803f80u, a1); a2 = dot2(xr[1], xr[1], a2);
      s1 = dot2(xr[2], 0x3f803f80u, s1); s2 = dot2(xr[2], xr[2], s2); a1 = dot2(xr[3], 0x3f803f80u, a1); a2 = dot2(xr[3], xr[3], a2);
      s1 += a1; s2 += a2;
    }
    float r = red32(s1, s2);
    r += dppf<0x111, 0xf>(r); r += dppf<0x112, 0xf>(r); r += dppf<0x114, 0xf>(r); r += dppf<0x118, 0xf>(r); r += dppf<0x142, 0xa>(r);
    const float S = __int_as_float(__builtin_amdgcn_readlane(__float_as_int(r), 31)), Q = __int_as_float(__builtin_amdgcn_readlane(__float_as_int(r), 63));
    u32x2 g = {stat_payload(S, Q), tag};
    if (lane == 0) { u32 o = OFF_STG + row * 8; ST8S(o, g, ws); }
    BAR();
    const int j = 112 * w + (lane < 56 ? lane : 55);
    const u32* src = (const u32*)(smem + LDS_TX) + (j & 3) * 456 + (j >> 2);
    const u32 o = OFF_XG + (u32)(j >> 2) * (L_ * 8) + b * 64 + (j & 3) * 16;
    u32x4 v0 = {src[0], tag, src[228], tag}, v1 = {src[14], tag, src[242], tag};
    ST16S(o, v0, ws); ST16S(o, v1, ws + 14 * (L_ * 8));
  };

  if (!is_main) {
    LN_LOAD();
    const int pid = xcd * 4 + (bi - 28);
    const int pq = pid < 28 ? pid : 0;
    const int wk = w < 7 ? w : 6;
    constexpr int PST = 49152;
    const u32 lst = lbase + PST + w * 14336;
    u32 craw;
    { u32 o = (t < 224 ? t : 0) * 4; LD4(craw, o, p.c); }
    {
      const u32 o = (u32)(64 * wk + (lane >> 2)) * (6 * D_ * 2) + pq * 64 + (lane & 3) * 16;
#pragma unroll
      for (int i = 0; i < 4; i++) { const char* bs = (const char*)p.wm + (size_t)i * 16 * (6 * D_ * 2); u32 l_ = lst + i * 1024; DMA(o, bs, l_); }
    }
    asm volatile("s_waitcnt vmcnt(4)" : "+v"(xr), "+v"(ep), "+v"(craw) :: "memory");
    const u32 tag = rfl(ep) * 1664525u + 1013904223u;
    u32* vec = (u32*)(smem);
    float* red = (float*)(smem + 2048);
    if (t < 224) {
      float a = lo_f(craw), c2 = hi_f(craw);
      vec[t] = pack2(a / (1.f + fexp(-a)), c2 / (1.f + fexp(-c2)));
    }
    ln_publish(xr, tag);
    const char* brd = smem + PST + w * 14336 + (lane >> 5) * 512 + ((lane & 15) >> 2) * 64 + ((lane >> 4) & 1) * 32 + (lane & 3) * 8;
    const u32 wl_row = lane >> 3, wl_col = ((lane & 7) ^ ((lane >> 2) & 4)) * 16;
    const char* crd0 = smem + PST + w * 14336 + (lane >> 5) * 1024 + ((lane & 15) >> 2) * 128 + ((lane >> 4) & 1) * 32 + (lane & 3) * 8;
    const char* crd[2] = {crd0 + ((lane >> 3) & 1) * 64, crd0 + (((lane >> 3) & 1) ^ 1) * 64};
    u32x4 A[4];
#pragma unroll
    for (int i = 0; i < 4; i++) A[i] = *(const u32x4*)((const char*)vec + (4 * wk + i) * 32 + (lane >> 5) * 16);
    {
      WAITV(3);
      f32x16 acc = {};
#pragma unroll
      for (int i = 0; i < 4; i++) acc = __builtin_amdgcn_mfma_f32_32x32x16_bf16(asb(A[i]), asb(tr_frag(brd + i * 1024, brd + i * 1024 + 256)), acc, 0, 0, 0);
      if (lane < 32) red[w * 256 + lane] = acc[0];
      const u32 o = (u32)(64 * wk + wl_row) * (6 * D_ * 2) + (896 + 64 * pq) * 2 + wl_col;
      BAR();
      if (t < 16 && pid < 28) {
        f32x2 s = {0.f, 0.f};
#pragma unroll
        for (int i = 0; i < 7; i++) s += *(const f32x2*)(red + i * 256 + 2 * t);
        u32x2 gr = {pk2(s), tag};
        u32 oo = OFF_MODG + (u32)(16 * pq + t) * 8; ST8S(oo, gr, ws);
      }
      STAMP(1);
#pragma unroll
      for (int i = 0; i < 8; i++) { const char* bs = (const char*)p.wm + (size_t)i * 8 * (6 * D_ * 2); u32 l_ = lst + i * 1024; DMA(o, bs, l_); }
      WAITV(0);
#pragma unroll
      for (int h = 0; h < 2; h++) {
        f32x16 a2 = {};
#pragma unroll
        for (int i = 0; i < 4; i++) a2 = __builtin_amdgcn_mfma_f32_32x32x16_bf16(asb(A[i]), asb(tr_frag(crd[h] + i * 2048, crd[h] + i * 2048 + 512)), a2, 0, 0, 0);
        if (lane < 32) red[w * 256 + 64 + h * 32 + lane] = a2[0];
      }
      BAR();
      if (t < 32 && pid < 28) {
        f32x2 s = {0.f, 0.f};
#pragma unroll
        for (int i = 0; i < 7; i++) s += *(const f32x2*)(red + i * 256 + 64 + 2 * t);
        u32x2 gr = {pk2(s), tag};
        u32 oo = OFF_MODG + (u32)(448 + 32 * pq + t) * 8; ST8S(oo, gr, ws);
      }
    }
    STAMP(2);
    if (w < 4 && pid < 28) {
      const int tau = pid * 4 + w;
      const u32 o = (u32)(16 * tau + wl_row) * (D_ * 2) + wl_col;
#pragma unroll
      for (int i = 0; i < 14; i++) { const char* bs = (const char*)p.w2 + (i & 1) * 8 * (D_ * 2) + (i >> 1) * 128; u32 l_ = lst + i * 1024; DMA(o, bs, l_); }
      WAITV(0);
      const u32 oo = OFF_W2T + (u32)(tau * 64 + lane) * 16;
#pragma unroll
      for (int i = 0; i < 14; i++) { u32x4 f = tr_frag(crd[i & 1] + (i >> 1) * 2048, crd[i & 1] + (i >> 1) * 2048 + 512); const char* bs = ws + i * 112 * 1024; ST16S(oo, f, bs); }
    }
    STAMP(6);
    {
      u32x4 g0, g1; u32x2 gt;
      const u32 og = OFF_STG + t * 32, o_gt = OFF_MODG + (u32)(3 * 224 + (t < 448 ? t : 447)) * 8;
      const u32 o = (u32)(64 * wk + wl_row) * (F_ * 2) + pq * 128 + wl_col;
#pragma unroll
      for (int i = 0; i < 8; i++) { const char* bs = (const char*)p.w1 + (size_t)i * 8 * (F_ * 2); u32 l_ = lst + i * 1024; DMA(o, bs, l_); }
#pragma nounroll
      for (int sp = 0; sp < SPIN_CAP; sp++) {
        asm volatile("global_load_dwordx4 %0, %3, %5 sc1\n\tglobal_load_dwordx4 %1, %3, %5 offset:16 sc1\n\tglobal_load_dwordx2 %2, %4, %5 sc1\n\ts_waitcnt vmcnt(0)"
                     : "=&v"(g0), "=&v"(g1), "=&v"(gt) : "v"(og), "v"(o_gt), "s"(ws) : "memory");
        if (__all(g0[1] == tag && g0[3] == tag && g1[1] == tag && g1[3] == tag && gt[1] == tag)) break;
        __builtin_amdgcn_s_sleep(4);
      }
      if (t < 448) {
        u32 v = gt[0];
        if (t >= 224) v = pack2(1.f + lo_f(v), 1.f + hi_f(v));
        vec[t] = v;
      }
      BAR();
      if (t == 0) { u32 of = OFF_W2F + pid * 4; ST4S(of, tag, ws); }
#pragma unroll
      for (int i = 0; i < 4; i++) A[i] = *(const u32x4*)((const char*)vec + ((lane & 1) ? 0 : 896) + (4 * wk + i) * 32 + (lane >> 5) * 16);
#pragma unroll
      for (int m = 0; m < 2; m++) {
        u32x4 Bf[8];
#pragma unroll
        for (int i = 0; i < 8; i++) Bf[i] = tr_frag(crd[i >> 2] + (i & 3) * 2048, crd[i >> 2] + (i & 3) * 2048 + 512);
        if (m == 0) {
#pragma unroll
          for (int i = 0; i < 8; i++) PIN(Bf[i]);
#pragma unroll
          for (int i = 0; i < 8; i++) { const char* bs = (const char*)p.w3 + (size_t)i * 8 * (F_ * 2); u32 l_ = lst + i * 1024; DMA(o, bs, l_); }
        }
#pragma unroll
        for (int h = 0; h < 2; h++) {
          f32x16 acc = {};
#pragma unroll
          for (int i = 0; i < 4; i++) acc = __builtin_amdgcn_mfma_f32_32x32x16_bf16(asb(A[i]), asb(Bf[4 * h + i]), acc, 0, 0, 0);
          if (lane < 32) { red[w * 256 + (2 * m + h) * 64 + lane] = acc[0]; red[w * 256 + (2 * m + h) * 64 + 32 + lane] = acc[1]; }
        }
        if (m == 0) WAITV(0);
      }
      BAR();
      if (t < 256 && pid < 28) {
        float s = 0.f;
#pragma unroll
        for (int i = 0; i < 7; i++) s += red[i * 256 + t];
        u32x2 gr = {__float_as_uint(s), tag};
        u32 oo = OFF_CSG + (u32)(((t >> 5) & 1) * 3584 + (t >> 7) * F_ + pid * 64 + ((t >> 6) & 1) * 32 + (t & 31)) * 8; ST8S(oo, gr, ws);
      }
    }
    STAMP(3);
    if (w == 0) poll_eq14(ws, OFF_CTR + (u32)(xcd * 16 + (lane & 15)) * 128);
    BAR();
    STAMP(4);
    {
      u32x4 v[7];
      u32 o = OFF_X1T + (u32)(t >> 3) * (L_ * 2) + pid * 128 + (t & 7) * 16;
#pragma unroll
      for (int i = 0; i < 7; i++) { const char* bs = ws + (size_t)i * 64 * (L_ * 2); LD16S(v[i], o, bs); }
      asm volatile("s_waitcnt vmcnt(0)" : "+v"(v[0]), "+v"(v[1]), "+v"(v[2]), "+v"(v[3]), "+v"(v[4]), "+v"(v[5]), "+v"(v[6]) :: "memory");
      f32x2 s1[4] = {}, s2[4] = {};
#pragma unroll
      for (int i = 0; i < 7; i++)
#pragma unroll
        for (int q = 0; q < 4; q++) { const f32x2 a = up2(v[i][q]); s1[q] += a; s2[q] = __builtin_elementwise_fma(a, a, s2[q]); }
      float a8[8], a4[4];
#pragma unroll
      for (int q = 0; q < 8; q++) a8[q] = red32(s1[q >> 1][q & 1], s2[q >> 1][q & 1]);
#pragma unroll
      for (int q = 0; q < 4; q++) a4[q] = red16(a8[q], a8[q + 4]);
#pragma unroll
      for (int q = 0; q < 4; q++) a4[q] += dppf<0x128, 0xf>(a4[q]);
      float* rd2 = (float*)smem;
      if (!(lane & 8)) *(f32x4*)(rd2 + (w * 2 + (lane >> 5)) * 64 + (lane & 7) * 8 + ((lane >> 4) & 1) * 4) = (f32x4){a4[0], a4[1], a4[2], a4[3]};
      BAR();
      if (t < 64) {
        float S = 0.f, Q = 0.f;
#pragma unroll
        for (int i = 0; i < 8; i++) { S += rd2[i * 128 + t]; Q += rd2[i * 128 + 64 + t]; }
        u32x2 gr = {stat_payload(S, Q), tag};
        u32 oo = OFF_ST2G + (pid * 64 + t) * 8; ST8S(oo, gr, ws);
      }
    }
    STAMP(5);
    STAMP_OUT;
    return;
  }

  LN_LOAD();
  const int id = xcd * 28 + bi;
  const int pm = (xcd >> 1) * 4 + (bi & 3), cg = (xcd & 1) * 7 + (bi >> 2);
  const int kch = w >> 2, tt = t & 255;
  u32 r_lar, r_aim, r_cr, r_ci, r_ldt, r_dsk;
  {
    u32 o = (u32)((2 * id + kch) * N_ + tt) * 2;
    LD2(r_lar, o, p.lar); LD2(r_aim, o, p.aim); LD2(r_cr, o, p.cr); LD2(r_ci, o, p.ci);
    u32 o2 = (2 * id + kch) * 2; LD2(r_ldt, o2, p.ldt); LD2(r_dsk, o2, p.dsk);
  }
  BAR_RAW();
  const uint64_t wlane = (uint64_t)(((lane >> 3) & 1) ? p.w3 : p.w1) + (size_t)(4 * (w >> 1) + (lane >> 4)) * (F_ * 2)
                         + (128 * cg + 64 * (w & 1) + 16 * (((lane >> 1) & 3) ^ (lane >> 4)) + 8 * (lane & 1)) * 2;
#define DMA_W(rows, ldsoff) do { uint64_t a_ = wlane + (int64_t)(rows) * (F_ * 2); u32 l_ = lbase + (ldsoff) + w * 1024; DMA64(a_, l_); } while (0)
#pragma unroll
  for (int s = 0; s < 4; s++) DMA_W(16 * s, LDS_S1 + s * 8192);
  u32 gix[6];
#pragma unroll
  for (int q = 0; q < 6; q++) { gix[q] = (u32)(2 * qidx(8 * tt - 4 + 2 * q)) | ((u32)(2 * qidx(8 * tt - 3 + 2 * q)) << 16); PIN(gix[q]); }
  asm volatile("s_waitcnt vmcnt(10)" : "+v"(xr), "+v"(ep) :: "memory");
  const u32 tag = rfl(ep) * 1664525u + 1013904223u;
  ln_publish(xr, tag);
  const u32 o_x1 = (u32)(2 * id + kch) * (L_ * 2) + tt * 16;
  STAMP(1);
  asm volatile("s_waitcnt vmcnt(%6)" : "+v"(r_lar), "+v"(r_aim), "+v"(r_cr), "+v"(r_ci), "+v"(r_ldt), "+v"(r_dsk) : "i"(4 + 3) : "memory");
  u32x4 W[28];
  const char* wrd = smem + (w >> 2) * 1024 + (lane >> 5) * 4096 + ((lane & 15) >> 2) * 256 + ((lane >> 4) & 1) * 128
                    + ((w & 3) ^ ((lane & 15) >> 2)) * 32 + (((lane & 1) << 1) | ((lane >> 1) & 1)) * 8;
  {
    const float dt = rb(fexp(lo_f(r_ldt)));
    const float a2 = -rb(fexp(lo_f(r_lar))) * dt * 1.44269504088896341f;
    const float bb = lo_f(r_aim) * dt;
    const float bh = bb * 0.15915494309189535f;
    const float bl = __builtin_fmaf(bb, 0.15915494309189535f, -bh) + bb * 6.4206382e-9f;
    auto zpow = [&](float pw) {
      float m = __builtin_amdgcn_exp2f(a2 * pw);
      float th = __builtin_amdgcn_fractf(bh * pw) + bl * pw;
      return (f32x2){m * __builtin_amdgcn_cosf(th), m * __builtin_amdgcn_sinf(th)};
    };
    const f32x2 z1v = zpow(1.f), z8v = zpow(8.f), z128v = zpow(128.f);
    const cm z1 = mk_cm(z1v[0], z1v[1]), z8c = mk_cm(z8v[0], -z8v[1]), z128 = mk_cm(z128v[0], z128v[1]);
    const float c_r = lo_f(r_cr), c_i = lo_f(r_ci);
    int ex = __builtin_amdgcn_frexp_expf(wave_max(fmaxf(fabsf(c_r), fabsf(c_i))));
    ex = ex < -90 ? -90 : ex;
    if (lane == 0) *(float*)(smem + LDS_CNT + 16 + 4 * w) = __builtin_amdgcn_ldexpf(2.f * dt, ex - 28);
    char* buf = smem + LDS_KG + w * KGW;
    const u32 m0v = lbase + LDS_KG + w * KGW;
    const int c = lane & 15, g = lane >> 4;
    const char* rd = buf + (c < 8 ? c : (c ^ 4)) * 272 + ((g & 1) * 8 + (g >> 1) * 4) * 16;
    DMA_W(16 * 4, LDS_S1 + 4 * 8192);
    f32x2 x[8];
    u32 v[8];
    x[0] = (f32x2){__builtin_amdgcn_ldexpf(c_r, 14 - ex), __builtin_amdgcn_ldexpf(c_i, 14 - ex)};
    f32x2 y = {16384.f, 0.f};
    v[0] = packh(16384.f, 0.f);
#pragma unroll
    for (int i = 1; i < 8; i++) { y = cmul(y, z8c); v[i] = pkh(y); x[i] = cmul(x[i - 1], z1); }
    wrt8<0>(m0v, v);
    DMA_W(16 * 5, LDS_S1 + 5 * 8192);
    u32 v2[8], v3[8];
#pragma unroll
    for (int i = 0; i < 8; i++) { y = cmul(y, z8c); v[i] = pkh(y); v2[i] = pkh(x[i]); x[i] = cmul(x[i], z128); v3[i] = pkh(x[i]); }
    wrt8<8>(m0v, v);
    u32x4 bf[4], af[4];
#pragma unroll
    for (int s = 0; s < 4; s++) bf[s] = *(const u32x4*)(rd + 16 * s);
    wrt8<0>(m0v, v2); wrt8<8>(m0v, v3);
    f32x4 acc[3];
    auto rdA = [&]() {
#pragma unroll
      for (int s = 0; s < 4; s++) af[s] = *(const u32x4*)(rd + 16 * s);
    };
    auto mm = [&](int T) {
      f32x4 a = {0, 0, 0, 0};
#pragma unroll
      for (int s = 0; s < 4; s++) a = __builtin_amdgcn_mfma_f32_16x16x32_f16(ash(af[s]), ash(bf[s]), a, 0, 0, 0);
      acc[T] = a;
    };
    rdA();
    DMA_W(16 * 6, LDS_S1 + 6 * 8192); DMA_W(16 * 7, LDS_S1 + 7 * 8192);
#pragma unroll
    for (int r = 0; r < 2; r++)
#pragma unroll
      for (int i = 0; i < 4; i++) { x[2 * i] = cmul(x[2 * i], z128); v[4 * r + i] = pkh(x[2 * i]); }
#pragma unroll
    for (int r = 0; r < 4; r++)
#pragma unroll
      for (int i = 0; i < 2; i++) { x[4 * i] = cmul(x[4 * i], z128); v2[2 * r + i] = pkh(x[4 * i]); }
    mm(0);
    wrt8<0>(m0v, v); wrt8<8>(m0v, v2);
    rdA();
    DMA_W(16 * 8, LDS_S1 + 8 * 8192); DMA_W(16 * 9, LDS_S1 + 9 * 8192);
    {
      const f32x2 q = cmul(z128v, z128);
      const cm z256 = mk_cm(q[0], q[1]);
      f32x2 e = cmul(x[0], z128), o = cmul(x[0], z256);
      v[0] = pkh(e); v[1] = pkh(o);
#pragma unroll
      for (int r = 1; r < 4; r++) { e = cmul(e, z256); o = cmul(o, z256); v[2 * r] = pkh(e); v[2 * r + 1] = pkh(o); }
      e = cmul(e, z256);
      mm(1);
      wrt8<0>(m0v, v);
      *(u32*)(buf + rpos(8) + lane * 4) = pkh(e);
    }
    __builtin_amdgcn_wave_barrier();
    rdA();
    DMA_W(16 * 10, LDS_S1 + 10 * 8192); DMA_W(16 * 11, LDS_S1 + 11 * 8192); DMA_W(16 * 12, LDS_S1 + 12 * 8192);
    mm(2);
    float* tab = (float*)buf;
    *(f32x4*)(tab + 4 * (g & 1) + 128 * (g >> 1) + 8 * c) = acc[0];
    const int q1 = g < 2 ? 256 + 64 * g + 4 * c : 384 + 64 * (g - 2) + 2 * c, q1d = g < 2 ? 2 : 32;
    *(f32x2*)(tab + q1) = (f32x2){acc[1][0], acc[1][1]};
    *(f32x2*)(tab + q1 + q1d) = (f32x2){acc[1][2], acc[1][3]};
#pragma unroll
    for (int r = 0; r < 4; r++) tab[512 + 16 * (4 * g + r) + c] = acc[2][r];
  }
  STAMP(2);
  u32x4 g0, g1, xa, xb; u32x2 gm;
  const u32 o_g = OFF_STG + t * 32, o_xg = OFF_XG + (u32)id * (L_ * 8) + t * 32;
  const u32 o_gm = OFF_MODG + (u32)((lane & 1) * 224 + id) * 8;
  asm volatile("global_load_dwordx4 %0, %5, %8 sc1\n\tglobal_load_dwordx4 %1, %5, %8 offset:16 sc1\n\tglobal_load_dwordx4 %2, %6, %8 sc1\n\tglobal_load_dwordx4 %3, %6, %8 offset:16 sc1\n\tglobal_load_dwordx2 %4, %7, %8 sc1"
               : "=&v"(g0), "=&v"(g1), "=&v"(xa), "=&v"(xb), "=&v"(gm) : "v"(o_g), "v"(o_xg), "v"(o_gm), "s"(ws) : "memory");
  WAITV(5);
  BAR();
  STAMP(3);
  {
    u32x4 z = {0, 0, 0, 0};
    ST16S(o_x1, z, ws + OFF_X1T); ST16S(o_x1, z, ws + OFF_X1S);
  }
#pragma unroll
  for (int s = 0; s < R1; s++) W[s] = tr_frag(wrd + LDS_S1 + s * 8192, wrd + LDS_S1 + s * 8192 + 2048);
  if (tt < 161) {
    const char* pp = smem + LDS_KG + kch * 4 * KGW + tt * 16;
    const f32x4 sc = *(const f32x4*)(smem + LDS_CNT + 16 + kch * 16);
    f32x4 v = *(const f32x4*)pp * sc[0];
#pragma unroll
    for (int i = 1; i < 4; i++) v = __builtin_elementwise_fma(*(const f32x4*)(pp + i * KGW), (f32x4){sc[i], sc[i], sc[i], sc[i]}, v);
    if (tt == 160) v[1] = 0.f;
    *(u32x2*)(smem + LDS_T16 + kch * 1408 + tt * 8) = (u32x2){pack2(v[0], v[1]), pack2(v[2], v[3])};
  }
  BAR();
  STAMP(17);
  {
    const char* t16 = smem + LDS_T16 + kch * 1408;
    u32 wv[6];
#pragma unroll
    for (int q = 0; q < 6; q++) wv[q] = (u32)*(const u16*)(t16 + (gix[q] & 0xffffu)) | ((u32)*(const u16*)(t16 + (gix[q] >> 16)) << 16);
    u32 al[5];
#pragma unroll
    for (int i = 0; i < 5; i++) al[i] = __builtin_amdgcn_alignbit(wv[i + 1], wv[i], 16);
    char* base = smem + LDS_CV + kch * CH_BYTES + 64 + 16 * tt;
    *(u32x4*)(base) = (u32x4){wv[2], wv[3], wv[4], wv[5]};
    *(u32x4*)(base + 4160) = (u32x4){al[1], al[2], al[3], al[4]};
    *(u32x4*)(base + 2 * 4160) = (u32x4){wv[1], wv[2], wv[3], wv[4]};
    *(u32x4*)(base + 3 * 4160) = (u32x4){al[0], al[1], al[2], al[3]};
    if (tt < 48) {
      const int zo = tt < 32 ? KC_BYTES + ((tt >> 4) * 144 + (tt & 15)) * 16 : ((tt >> 2) & 3) * 4160 + (tt & 3) * 16;
      *(u32x4*)(smem + LDS_CV + kch * CH_BYTES + zo) = (u32x4){0, 0, 0, 0};
    }
  }
  STAMP(16);
  asm volatile("s_waitcnt vmcnt(2)" : "+v"(g0), "+v"(g1), "+v"(xa), "+v"(xb), "+v"(gm) :: "memory");
#pragma nounroll
  for (int sp = 0; sp < SPIN_CAP; sp++) {
    const u32 bad = ((g0[1] ^ tag) | (g0[3] ^ tag) | (g1[1] ^ tag)) | ((g1[3] ^ tag) | (xa[1] ^ tag) | (xa[3] ^ tag)) | ((xb[1] ^ tag) | (xb[3] ^ tag) | (gm[1] ^ tag));
    if (__all(bad == 0)) break;
    asm volatile("global_load_dwordx4 %0, %5, %8 sc1\n\tglobal_load_dwordx4 %1, %5, %8 offset:16 sc1\n\tglobal_load_dwordx4 %2, %6, %8 sc1\n\tglobal_load_dwordx4 %3, %6, %8 offset:16 sc1\n\tglobal_load_dwordx2 %4, %7, %8 sc1\n\ts_waitcnt vmcnt(0)"
                 : "=&v"(g0), "=&v"(g1), "=&v"(xa), "=&v"(xb), "=&v"(gm) : "v"(o_g), "v"(o_xg), "v"(o_gm), "s"(ws) : "memory");
  }
  STAMP(4);
  {
    const u32 m_sa = __builtin_amdgcn_readlane(gm[0], 0), m_ca = __builtin_amdgcn_readlane(gm[0], 1);
    const f32x2 c1 = up2(pk2(up2(m_ca) + 1.f)), sa = up2(m_sa);
    const u32 xg[4] = {xa[0], xa[2], xb[0], xb[2]}, pl[4] = {g0[0], g0[2], g1[0], g1[2]};
    u32 hq[4];
#pragma unroll
    for (int i = 0; i < 4; i++) {
      const float m = lo_f(pl[i]), rs = __builtin_amdgcn_rcpf(hi_f(pl[i]));
      const f32x2 n = up2(pk2((up2(xg[i]) - m) * rs));
      hq[i] = pk2(up2(pk2(n * c1)) + sa);
    }
    const int po = (((t >> 1) & 1) * 144 + 16 + (t >> 2)) * 16 + (t & 1) * 8;
    char* hb = smem + LDS_CV + KC_BYTES + po;
    *(u32x2*)(hb) = (u32x2){__builtin_amdgcn_perm(hq[1], hq[0], 0x05040100u), __builtin_amdgcn_perm(hq[3], hq[2], 0x05040100u)};
    *(u32x2*)(hb + CH_BYTES) = (u32x2){__builtin_amdgcn_perm(hq[1], hq[0], 0x07060302u), __builtin_amdgcn_perm(hq[3], hq[2], 0x07060302u)};
    char* xb_ = smem + LDS_Y + 8 * t;
    *(u32x2*)(xb_) = (u32x2){__builtin_amdgcn_perm(xg[1], xg[0], 0x05040100u), __builtin_amdgcn_perm(xg[3], xg[2], 0x05040100u)};
    *(u32x2*)(xb_ + 4096) = (u32x2){__builtin_amdgcn_perm(xg[1], xg[0], 0x07060302u), __builtin_amdgcn_perm(xg[3], xg[2], 0x07060302u)};
  }
  BAR();
  if (b == 0 && t == 0) { u32 z = 0; ST4S(z, tag, ws); }
  STAMP(5);
  const u32x4 xo_raw = *(const u32x4*)(smem + LDS_Y + kch * 4096 + 16 * tt);
  const u32x4 ho_raw = *(const u32x4*)(smem + LDS_CV + kch * CH_BYTES + KC_BYTES + ((tt & 1) * 144 + 16 + (tt >> 1)) * 16);
  u32x2 gm2;
  const u32 o_gm2 = OFF_MODG + (u32)(((lane & 1) ? 4 : 2) * 224 + id) * 8;
  LD8S(gm2, o_gm2, ws);
  BAR();
  if (w < 4) {
    const int ch = w >> 1, hf = w & 1, r = lane & 15, g = lane >> 4;
    const char* ab = smem + LDS_CV + ch * CH_BYTES + (r >> 2) * 4160 + 2 * (32 + 16 * (1 + 8 * hf) - 8 * g - 4 + 4 * (r & 3));
    const char* bb = smem + LDS_CV + ch * CH_BYTES + KC_BYTES + ((g & 1) * 144 + 16 + r + (g >> 1) - 7 - 8 * hf) * 16;
    f32x4 acc[8] = {};
    u32x4 A[2][8], B[2][8];
    auto ld = [&](int j, int u) {
#pragma unroll
      for (int a = 0; a < 8; a++) {
        const int o = 512 * a + 64 * j;
        A[u][a] = tr_frag(ab + o + 8, ab + o);
        B[u][a] = *(const u32x4*)(bb + 256 * a + 32 * (3 - j));
      }
    };
    ld(0, 0);
    SCHED();
#pragma unroll
    for (int j = 0; j < 4; j++) {
      const int u = j & 1;
      if (j < 3) ld(j + 1, u ^ 1);
#pragma unroll
      for (int a = 0; a < 8; a++)
#pragma unroll
        for (int nt = a; nt < 8; nt++)
          acc[nt] = __builtin_amdgcn_mfma_f32_16x16x32_bf16(asb(A[u][a]), asb(B[u][nt - a]), acc[nt], 0, 0, 0);
#pragma unroll
      for (int i = 0; i < 36; i++) { __builtin_amdgcn_sched_group_barrier(0x8, 1, 0); __builtin_amdgcn_sched_group_barrier(0x100, 1, 0); }
      SCHED();
    }
    const int u0 = 4 * r + g;
    char* yw = smem + LDS_Y + (hf * 2 + ch) * 8192 + (u0 ^ ((u0 >> 3) & 7)) * 16;
#pragma unroll
    for (int nt = 0; nt < 8; nt++) *(f32x4*)(yw + nt * 1024) = acc[nt];
  } else {
#pragma unroll
    for (int s = 0; s < R2; s++) { DMA_W(16 * (R1 + s), s * 8192); DMA_W(16 * (R1 + s) - 8, s * 8192 - 4096); }
  }
  STAMP(6);
  asm volatile("s_waitcnt vmcnt(0)" : "+v"(gm2) :: "memory");
#pragma nounroll
  for (int sp = 0; sp < SPIN_CAP; sp++) {
    if (__all(gm2[1] == tag)) break;
    asm volatile("global_load_dwordx2 %0, %1, %2 sc1\n\ts_waitcnt vmcnt(0)" : "=&v"(gm2) : "v"(o_gm2), "s"(ws) : "memory");
  }
  STAMP(19);
  BAR();
  STAMP(20);
  {
    const char* yr = smem + LDS_Y + kch * 8192;
    const int ua = (2 * tt) ^ ((tt >> 2) & 7);
    const f32x4 ya = *(const f32x4*)(yr + ua * 16) + *(const f32x4*)(yr + 16384 + ua * 16);
    const f32x4 yb = *(const f32x4*)(yr + (ua ^ 1) * 16) + *(const f32x4*)(yr + 16384 + (ua ^ 1) * 16);
    const u32x4 y = {pack2(ya[0], ya[1]), pack2(ya[2], ya[3]), pack2(yb[0], yb[1]), pack2(yb[2], yb[3])};
    const u32 m_ga = __builtin_amdgcn_readlane(gm2[0], 0), m_cb = __builtin_amdgcn_readlane(gm2[0], 1);
    const float ga = kch ? hi_f(m_ga) : lo_f(m_ga), c1b = rb(1.f + (kch ? hi_f(m_cb) : lo_f(m_cb))), dk = lo_f(r_dsk);
    u32x4 ox, os;
#pragma unroll
    for (int q = 0; q < 4; q++) {
      const f32x2 hd = up2(pk2(up2(ho_raw[q]) * dk));
      const f32x2 a = up2(pk2(up2(y[q]) + hd));
      const f32x2 m = up2(pk2(a * ga));
      ox[q] = pk2(up2(xo_raw[q]) + m);
      os[q] = pk2(up2(ox[q]) * c1b);
    }
    ST16S(o_x1, os, ws + OFF_X1S); ST16S(o_x1, ox, ws + OFF_X1T);
  }
#pragma unroll
  for (int s = 0; s < R2; s++) W[R1 + s] = tr_frag(wrd + s * 8192, wrd + s * 8192 + 2048);
  STAMP(21);
  WAITV(0);
  STAMP(22);
  BAR();
  if (t < 8) { u32 o = OFF_CTR + (u32)(t * 16 + (id & 15)) * 128; ATOM_INC(o, ws); }
  STAMP(7);
#pragma unroll
  for (int s = 0; s < R3; s++) DMA_W(16 * (R1 + R2 + s), LDS_R3 + s * 8192);
  if (w == 7) {
    const u32 off = lane < 32 ? OFF_CTR + (u32)(xcd * 16 + (lane & 15)) * 128 : OFF_W2F + (lane & 31) * 4;
    const u32 want = lane < 32 ? 14u : tag;
#pragma nounroll
    for (int sp = 0; sp < SPIN_CAP; sp++) {
      u32 v; asm volatile("global_load_dword %0, %1, %2 sc1\n\ts_waitcnt vmcnt(0)" : "=&v"(v) : "v"(off), "s"(ws) : "memory");
      if (__all(v == want)) break;
    }
  }
  WAITV(0);
  BAR();
  STAMP(8);
  const u32 o_xp = OFF_X1S + (u32)(8 * w + (lane >> 3)) * (L_ * 2) + (128 * pm) * 2 + ((lane & 7) ^ ((lane >> 2) & 4)) * 16;
#define DMA_X(i) do { const char* bs_ = ws + (size_t)((i) % 7) * 64 * (L_ * 2) + ((i) / 7) * 128; u32 l_ = lbase + (i) * 8192 + w * 1024; DMAS(o_xp, bs_, l_); } while (0)
#pragma unroll
  for (int i = 0; i < 4; i++) DMA_X(i);
#pragma unroll
  for (int s = 0; s < R3; s++) W[R1 + R2 + s] = tr_frag(wrd + LDS_R3 + s * 8192, wrd + LDS_R3 + s * 8192 + 2048);
  const char* xrd0 = smem + (lane >> 5) * 1024 + ((lane & 15) >> 2) * 128 + ((lane >> 4) & 1) * 32 + (lane & 3) * 8;
  const char* xrdA = xrd0 + ((lane >> 3) & 1) * 64;
  const char* xrdB = xrd0 + (((lane >> 3) & 1) ^ 1) * 64;
  const int p2 = 2 * xcd + ((bi >> 1) & 1), e2 = bi & 1, dg = bi >> 2;
  const u32 o_w2 = OFF_W2T + (u32)((2 * dg * 112 + 14 * w) * 64 + lane) * 16;
  const int rot = 2 * dg;
#define KROT(s) (((s) + rot >= 14 ? (s) + rot - 14 : (s) + rot) * 1024)
  u32x2 st2[4], csg;
  u32x4 Wx, W2[28];
  const u32 o_s2 = OFF_ST2G + (u32)(128 * pm + (lane & 31)) * 8;
  const u32 o_h = OFF_H + (u32)((pm * 4 * 112 + cg * 8 + w) * 64 + lane) * 16;
  const u32 o_cs = OFF_CSG + (u32)((lane >> 5) * 3584 + ((lane >> 4) & 1) * F_ + 128 * cg + 16 * w + ((lane & 3) | ((lane & 8) >> 1) | ((lane & 4) << 1))) * 8;
#define LD_W2(i) do { const char* bs_ = ws + ((i) & 1) * 112 * 1024 + KROT((i) >> 1); LD16S(W2[i], o_w2, bs_); } while (0)

  f32x16 a0 = {}, a1 = {}, a2 = {}, a3 = {};
  {
    u32x4 XA[3], XB[3];
    WAITV(3); BAR_RAW();
#pragma unroll
    for (int s = 0; s < 2; s++) { XA[s] = tr_frag(xrdA + s * 2048, xrdA + s * 2048 + 512); XB[s] = tr_frag(xrdB + s * 2048, xrdB + s * 2048 + 512); }
#pragma unroll
    for (int s = 0; s < 28; s++) {
      if (s + 2 < 28) {
        if (((s + 2) & 3) == 0) { WAITV(s == 22 ? 7 : (s + 2) / 4 + 2); BAR_RAW(); }
        XA[(s + 2) % 3] = tr_frag(xrdA + (s + 2) * 2048, xrdA + (s + 2) * 2048 + 512);
        XB[(s + 2) % 3] = tr_frag(xrdB + (s + 2) * 2048, xrdB + (s + 2) * 2048 + 512);
      }
      a0 = __builtin_amdgcn_mfma_f32_32x32x16_bf16(asb(W[s]), asb(XA[s % 3]), a0, 0, 0, 0);
      a1 = __builtin_amdgcn_mfma_f32_32x32x16_bf16(asb(W[s]), asb(XB[s % 3]), a1, 0, 0, 0);
      PIN(a0); PIN(a1);
      if ((s & 1) == 0 && s <= 18) DMA_X(4 + s / 2);
      if (s >= 23) LD_W2(s - 23);
      SCHED();
    }
  }
  STAMP(23);
  asm volatile("global_load_dwordx2 %0, %5, %7 sc1\n\tglobal_load_dwordx2 %1, %5, %7 offset:256 sc1\n\tglobal_load_dwordx2 %2, %5, %7 offset:512 sc1\n\tglobal_load_dwordx2 %3, %5, %7 offset:768 sc1\n\tglobal_load_dwordx2 %4, %6, %7 sc1"
               : "=&v"(st2[0]), "=&v"(st2[1]), "=&v"(st2[2]), "=&v"(st2[3]), "=&v"(csg) : "v"(o_s2), "v"(o_cs), "s"(ws) : "memory");
  WAITV(10);
  BAR_RAW();
  STAMP(10);
  auto fin = [&](f32x16& acc, int mt) {
    const u32 pl = st2[mt][0];
    const u32 e0 = lane < 32 ? ((pl ^ 0x8000u) & 0xffffu) : (pl >> 16);
    const u32x4 ex = {e0 | (e0 << 16), e0, 0u, 0u};
    acc = __builtin_amdgcn_mfma_f32_32x32x16_bf16(asb(Wx), asb(ex), acc, 0, 0, 0);
  };
  auto part = [&](const f32x16& acc, int mt, int j) {
    const float sd = hi_f(st2[mt][0]);
    const float c1 = -1.44269504088896341f * __builtin_amdgcn_rcpf(sd), sq = sd * sd;
    const f32x2 gg = {acc[2 * j], acc[2 * j + 1]}, uu = {acc[8 + 2 * j], acc[9 + 2 * j]};
    const f32x2 ar = gg * c1;
    const f32x2 e = {__builtin_amdgcn_exp2f(ar[0]), __builtin_amdgcn_exp2f(ar[1])};
    const f32x2 den = __builtin_elementwise_fma(e, (f32x2){sq, sq}, (f32x2){sq, sq});
    const f32x2 rc = {__builtin_amdgcn_rcpf(den[0]), __builtin_amdgcn_rcpf(den[1])};
    return pk2(gg * uu * rc);
  };
  auto put = [&](u32x4 o, int mt) { const char* bs = ws + (size_t)mt * (112 * 1024); ST16S(o_h, o, bs); };
  auto signal3 = [&](int half) {
    if (lane == 0) {
      u32* cp = (u32*)(smem + LDS_CNT);
      u32 old = __hip_atomic_fetch_add(cp + half, 1u, __ATOMIC_RELAXED, __HIP_MEMORY_SCOPE_WORKGROUP);
      if (old == 7u) { u32 o = OFF_CTR + (u32)(128 + 2 * pm + half) * 128; ATOM_INC(o, ws); }
    }
  };
  {
    const char* xp = xrdA + 57344;
    u32x4 X[4], ho;
#pragma unroll
    for (int s = 0; s < 3; s++) X[s] = tr_frag(xp + s * 2048, xp + s * 2048 + 512);
    SCHED();
#pragma unroll
    for (int s = 0; s < 28; s++) {
      if (s + 3 < 28) X[(s + 3) & 3] = tr_frag(xp + (s + 3) * 2048, xp + (s + 3) * 2048 + 512);
      a2 = __builtin_amdgcn_mfma_f32_32x32x16_bf16(asb(W[s]), asb(X[s & 3]), a2, 0, 0, 0);
      PIN(a2);
      if (s == 9) {
        asm volatile("s_waitcnt vmcnt(0)" : "+v"(st2[0]), "+v"(st2[1]), "+v"(st2[2]), "+v"(st2[3]), "+v"(csg) :: "memory");
#pragma nounroll
        for (int sp = 0; sp < SPIN_CAP; sp++) {
          if (__all(st2[0][1] == tag && st2[1][1] == tag && st2[2][1] == tag && st2[3][1] == tag && csg[1] == tag)) break;
          asm volatile("global_load_dwordx2 %0, %5, %7 sc1\n\tglobal_load_dwordx2 %1, %5, %7 offset:256 sc1\n\tglobal_load_dwordx2 %2, %5, %7 offset:512 sc1\n\tglobal_load_dwordx2 %3, %5, %7 offset:768 sc1\n\tglobal_load_dwordx2 %4, %6, %7 sc1\n\ts_waitcnt vmcnt(0)"
                       : "=&v"(st2[0]), "=&v"(st2[1]), "=&v"(st2[2]), "=&v"(st2[3]), "=&v"(csg) : "v"(o_s2), "v"(o_cs), "s"(ws) : "memory");
        }
        const float tot = __uint_as_float(csg[0]);
        const float h0 = rb(tot), h1 = rb(tot - h0), h2 = tot - h0 - h1;
        Wx = (u32x4){pack2(h0, h1), pack2(h2, 0.f), 0u, 0u};
        fin(a0, 0); fin(a1, 1);
      }
      if (s >= 10 && (s & 1) == 0) LD_W2(5 + (s - 10) / 2);
      if (s >= 11 && s < 27 && (s & 1) == 1) {
        const int q = (s - 11) / 2;
        ho[q & 3] = q < 4 ? part(a0, 0, q) : part(a1, 1, q - 4);
        PIN(ho);
        if (q == 3) put(ho, 0);
        if (q == 7) put(ho, 1);
      }
      SCHED();
    }
  }
  STAMP(26);
  u32 s3v;
  const u32 o_s3 = OFF_CTR + (u32)(128 + 2 * p2) * 128;
  u32x4 Hf[14];
  const u32 o_hl = OFF_H + (u32)(((4 * p2 + e2) * 112 + 14 * w) * 64 + lane) * 16;
  {
    const char* xp = xrdB + 57344;
    u32x4 X[4], ho;
#pragma unroll
    for (int s = 0; s < 3; s++) X[s] = tr_frag(xp + s * 2048, xp + s * 2048 + 512);
    fin(a2, 2);
    SCHED();
#pragma unroll
    for (int s = 0; s < 28; s++) {
      if (s + 3 < 28) X[(s + 3) & 3] = tr_frag(xp + (s + 3) * 2048, xp + (s + 3) * 2048 + 512);
      a3 = __builtin_amdgcn_mfma_f32_32x32x16_bf16(asb(W[s]), asb(X[s & 3]), a3, 0, 0, 0);
      PIN(a3);
      if (s == 6) { WAITV(0); signal3(0); }
      if (s >= 7 && s < 28 && (s % 3) != 0) { constexpr int dummy = 0; (void)dummy; LD_W2(14 + (s - 7) - (s - 6) / 3); }
      if (s >= 2 && s < 14 && (s % 3) == 2) {
        const int q = (s - 2) / 3;
        ho[q] = part(a2, 2, q);
        PIN(ho);
        if (q == 3) put(ho, 2);
      }
      SCHED();
    }
    STAMP(28);
    fin(a3, 3);
#pragma unroll
    for (int q = 0; q < 4; q++) ho[q] = part(a3, 3, q);
    put(ho, 3);
  }
  STAMP(29);
  asm volatile("global_load_dword %0, %1, %2 sc1\n\ts_waitcnt vmcnt(0)" : "=&v"(s3v) : "v"(o_s3), "s"(ws) : "memory");
  signal3(1);
#pragma unroll
  for (int i = 0; i < 28; i++) PIN(W2[i]);
  if (!__all(s3v == 14u)) poll_eq14(ws, o_s3);
  STAMP(11);
#pragma unroll
  for (int s = 0; s < 14; s++) { const char* bs = ws + KROT(s); LD16S(Hf[s], o_hl, bs); }
  const int ocol = 64 * dg + 32 * (w >> 2) + 8 * (w & 3) + 4 * (lane >> 5);
  const int orow = 128 * p2 + 32 * e2 + (lane & 31);
  u32 xres[8]; u32x4 gbg;
  const u32 o_gb = OFF_MODG + (u32)(5 * 224 + (ocol >> 1)) * 8;
  {
    const u32 o = OFF_X1T + (u32)ocol * (L_ * 2) + orow * 2;
#pragma unroll
    for (int j = 0; j < 8; j++) { const char* bs = ws + (j & 3) * (L_ * 2) + (j >> 2) * 128; LD2S(xres[j], o, bs); }
    LD16S(gbg, o_gb, ws);
  }
  BAR_RAW();
  auto half = [&](auto HH) {
    constexpr int hf = decltype(HH)::value;
    f32x16 f0 = {}, f1 = {};
#pragma unroll
    for (int s = 0; s < 14; s++) {
      asm volatile("s_waitcnt vmcnt(%1)" : "+v"(Hf[s]) : "i"(13 - s + (hf == 0 ? 9 : 0)) : "memory");
      f0 = __builtin_amdgcn_mfma_f32_32x32x16_bf16(asb(W2[2 * s]), asb(Hf[s]), f0, 0, 0, 0);
      f1 = __builtin_amdgcn_mfma_f32_32x32x16_bf16(asb(W2[2 * s + 1]), asb(Hf[s]), f1, 0, 0, 0);
      PIN(f0); PIN(f1);
      SCHED();
    }
    if (hf == 0) {
      STAMP(12);
      poll_eq14(ws, OFF_CTR + (u32)(128 + 2 * p2 + 1) * 128);
#pragma unroll
      for (int j = 0; j < 8; j++) PIN(xres[j]);
      PIN(gbg);
#pragma nounroll
      for (int sp = 0; sp < SPIN_CAP; sp++) {
        if (__all(gbg[1] == tag && gbg[3] == tag)) break;
        asm volatile("global_load_dwordx4 %0, %1, %2 sc1\n\ts_waitcnt vmcnt(0)" : "=&v"(gbg) : "v"(o_gb), "s"(ws) : "memory");
      }
#pragma unroll
      for (int s = 0; s < 14; s++) { const char* bs = ws + 2 * 112 * 1024 + KROT(s); LD16S(Hf[s], o_hl, bs); }
      STAMP(30);
    } else { STAMP(13); }
    char* rbuf = smem + hf * 65536;
#pragma unroll
    for (int o = 0; o < 8; o++) {
      const f32x16 sv = (o >> 2) ? f1 : f0; const int i0 = 4 * (o & 3);
      *(f32x4*)(rbuf + (o * 8 + w) * 1024 + lane * 16) = (f32x4){sv[i0], sv[i0 + 1], sv[i0 + 2], sv[i0 + 3]};
    }
    BAR();
    f32x4 sum = *(const f32x4*)(rbuf + (w * 8) * 1024 + lane * 16);
#pragma unroll
    for (int i = 1; i < 8; i++) sum += *(const f32x4*)(rbuf + (w * 8 + i) * 1024 + lane * 16);
    u32x2 res;
#pragma unroll
    for (int j = 0; j < 2; j++) {
      const f32x2 f = up2(pack2(sum[2 * j], sum[2 * j + 1]));
      const f32x2 m = up2(pk2(f * up2(gbg[2 * j])));
      res[j] = pk2((f32x2){lo_f(xres[4 * hf + 2 * j]), lo_f(xres[4 * hf + 2 * j + 1])} + m);
    }
    return res;
  };
  u32x2 r0 = half(std::integral_constant<int, 0>{});
  u32x2 r1 = half(std::integral_constant<int, 1>{});
  *(u32x2*)(p.out + (size_t)orow * D_ + ocol) = r0;
  *(u32x2*)(p.out + (size_t)(orow + 64) * D_ + ocol) = r1;
  STAMP(14);
  STAMP_OUT;
}

extern "C" void kernel_launch(void* const* d_in, const int* in_sizes, int n_in,
                              void* d_out, int out_size, void* d_ws, size_t ws_size,
                              hipStream_t stream) {
  Params p;
  p.x = (const u16*)d_in[0]; p.lar = (const u16*)d_in[1]; p.aim = (const u16*)d_in[2];
  p.cr = (const u16*)d_in[3]; p.ci = (const u16*)d_in[4]; p.ldt = (const u16*)d_in[5]; p.dsk = (const u16*)d_in[6];
  p.w1 = (const u16*)d_in[8]; p.w3 = (const u16*)d_in[9]; p.w2 = (const u16*)d_in[10];
  p.c = (const u16*)d_in[11]; p.wm = (const u16*)d_in[12]; p.ws = (char*)d_ws; p.out = (u16*)d_out;
  fused_kernel<<<256, 512, 0, stream>>>(p);
}
```

```cpp
#include <hip/hip_runtime.h>
#include <stdint.h>
#include <type_traits>

typedef unsigned short u16;
typedef unsigned int u32;
typedef __attribute__((ext_vector_type(8))) __bf16 bf16x8;
typedef __attribute__((ext_vector_type(2))) __bf16 bf16x2;
typedef __attribute__((ext_vector_type(8))) _Float16 f16x8;
typedef __attribute__((ext_vector_type(2))) _Float16 f16x2;
typedef __attribute__((ext_vector_type(4))) short s16x4;
typedef __attribute__((ext_vector_type(2))) float f32x2;
typedef __attribute__((ext_vector_type(4))) float f32x4;
typedef __attribute__((ext_vector_type(16))) float f32x16;
typedef __attribute__((ext_vector_type(4))) u32 u32x4;
typedef __attribute__((ext_vector_type(2))) u32 u32x2;
#define LAS __attribute__((address_space(3)))
#define DEV __device__ __forceinline__

constexpr int L_ = 2048, D_ = 448, N_ = 256, F_ = 1792;
constexpr u32 OFF_E    = 0;
constexpr u32 OFF_CTR  = 4096;
constexpr u32 OFF_W2F  = OFF_CTR + 160 * 128;
constexpr u32 OFF_STG  = 32768;
constexpr u32 OFF_MODG = 49152;
constexpr u32 OFF_ST2G = 65536;
constexpr u32 OFF_CSG  = 81920;
constexpr u32 OFF_XG   = 147456;
constexpr u32 OFF_X1T  = OFF_XG + 224 * L_ * 8;
constexpr u32 OFF_X1S  = OFF_X1T + D_ * L_ * 2;
constexpr u32 OFF_W2T  = OFF_X1S + D_ * L_ * 2;
constexpr u32 OFF_H    = OFF_W2T + 14 * 112 * 1024;
constexpr size_t OFF_DBG = (size_t)64 << 20;
constexpr int SPIN_CAP = 1 << 14;

#define OPT_CODEPF 1
#define OPT_TLB 0
#define OPT_ROT 0
#define OPT_ROT2 1
#define OPT_PREW 1
#define OPT_TOUCH 1
#define OPT_PRIO 1
constexpr int R1 = 9, R2 = 14, R3 = 5;
constexpr int KGW = 4352;
constexpr int LDS_KG  = 0;
constexpr int LDS_T16 = 8 * KGW;
constexpr int LDS_TX  = LDS_T16 + 2816;
constexpr int KC_BYTES = 4 * 4160, HP_BYTES = 2 * 144 * 16;
constexpr int LDS_KC  = LDS_TX + 8192;
constexpr int LDS_HP  = LDS_KC + 2 * KC_BYTES;
constexpr int LDS_S1  = LDS_HP + 2 * HP_BYTES;
constexpr int LDS_CNT = 163840 - 128;
constexpr int LDS_TOTAL = 163840;
constexpr int LDS_R3  = 32768;
constexpr int LDS_JUNK = LDS_CNT - 256;
constexpr int r2slot(int s) { return s < 5 ? s * 8192 : LDS_S1 + (s - 5) * 8192; }
static_assert(5 * 8192 <= LDS_KC && LDS_S1 + 9 * 8192 <= LDS_CNT && LDS_R3 + R3 * 8192 <= LDS_S1 && R1 + R2 + R3 == 28 && (LDS_S1 & 15) == 0, "lds map");
constexpr int CODE_LINES = 300;

#define STAMP(i) SCHED()
#define STAMP_DECL do {} while (0)
#define STAMP_OUT do {} while (0)

DEV u32 pack2(float lo, float hi) { f32x2 v = {lo, hi}; bf16x2 b = __builtin_convertvector(v, bf16x2); return __builtin_bit_cast(u32, b); }
DEV u32 packh(float lo, float hi) { f32x2 v = {lo, hi}; f16x2 b = __builtin_convertvector(v, f16x2); return __builtin_bit_cast(u32, b); }
DEV float lo_f(u32 u) { return __uint_as_float(u << 16); }
DEV float hi_f(u32 u) { return __uint_as_float(u & 0xffff0000u); }
DEV float rb(float f) { return lo_f(pack2(f, f)); }
DEV float dot2(u32 a, u32 b, float c) { return __builtin_amdgcn_fdot2_f32_bf16(__builtin_bit_cast(bf16x2, a), __builtin_bit_cast(bf16x2, b), c, false); }
DEV bf16x8 asb(u32x4 v) { return __builtin_bit_cast(bf16x8, v); }
DEV f16x8 ash(u32x4 v) { return __builtin_bit_cast(f16x8, v); }
DEV float fexp(float x) { return __builtin_amdgcn_exp2f(x * 1.44269504088896341f); }
DEV u32 rfl(u32 v) { return __builtin_amdgcn_readfirstlane(v); }

template <int CTRL, int RMASK> DEV float dppf(float v) { return __int_as_float(__builtin_amdgcn_update_dpp(0, __float_as_int(v), CTRL, RMASK, 0xf, false)); }
DEV float wave_sum(float v) {
  v += dppf<0x111, 0xf>(v); v += dppf<0x112, 0xf>(v); v += dppf<0x114, 0xf>(v); v += dppf<0x118, 0xf>(v);
  v += dppf<0x142, 0xa>(v); v += dppf<0x143, 0xc>(v);
  return __int_as_float(__builtin_amdgcn_readlane(__float_as_int(v), 63));
}
template <int CTRL, int RMASK> DEV u32 dppmax(u32 v) { u32 o = (u32)__builtin_amdgcn_update_dpp(0, (int)v, CTRL, RMASK, 0xf, false); return o > v ? o : v; }
DEV u32 wave_maxu(u32 v) {
  v = dppmax<0x111, 0xf>(v); v = dppmax<0x112, 0xf>(v); v = dppmax<0x114, 0xf>(v); v = dppmax<0x118, 0xf>(v);
  v = dppmax<0x142, 0xa>(v); v = dppmax<0x143, 0xc>(v);
  return __builtin_amdgcn_readlane(v, 63);
}
DEV float red32(float a, float b) { auto r = __builtin_amdgcn_permlane32_swap(__float_as_uint(a), __float_as_uint(b), false, false); return __uint_as_float(r[0]) + __uint_as_float(r[1]); }
DEV float red16(float a, float b) { auto r = __builtin_amdgcn_permlane16_swap(__float_as_uint(a), __float_as_uint(b), false, false); return __uint_as_float(r[0]) + __uint_as_float(r[1]); }

#define LD16(d, off, base)    asm volatile("global_load_dwordx4 %0, %1, %2" : "=v"(d) : "v"(off), "s"(base) : "memory")
#define LD16S(d, off, base)   asm volatile("global_load_dwordx4 %0, %1, %2 sc1" : "=v"(d) : "v"(off), "s"(base) : "memory")
#define LD8S(d, off, base)    asm volatile("global_load_dwordx2 %0, %1, %2 sc1" : "=v"(d) : "v"(off), "s"(base) : "memory")
#define LD4(d, off, base)     asm volatile("global_load_dword %0, %1, %2" : "=v"(d) : "v"(off), "s"(base) : "memory")
#define LD4S(d, off, base)    asm volatile("global_load_dword %0, %1, %2 sc1" : "=v"(d) : "v"(off), "s"(base) : "memory")
#define LD2(d, off, base)     asm volatile("global_load_ushort %0, %1, %2" : "=v"(d) : "v"(off), "s"(base) : "memory")
#define LD2S(d, off, base)    asm volatile("global_load_ushort %0, %1, %2 sc1" : "=v"(d) : "v"(off), "s"(base) : "memory")
#define ST16S(off, v, base)   asm volatile("global_store_dwordx4 %0, %1, %2 sc1\n\ts_nop 1" :: "v"(off), "v"(v), "s"(base) : "memory")
#define ST8S(off, v, base)    asm volatile("global_store_dwordx2 %0, %1, %2 sc1" :: "v"(off), "v"(v), "s"(base) : "memory")
#define ST4S(off, v, base)    asm volatile("global_store_dword %0, %1, %2 sc1" :: "v"(off), "v"(v), "s"(base) : "memory")
#define ATOM_INC(off, base)   asm volatile("global_atomic_add %0, %1, %2" :: "v"(off), "v"(1u), "s"(base) : "memory")
#define DMA(off, base, lds)   asm volatile("s_mov_b32 m0, %2\n\ts_nop 0\n\tglobal_load_lds_dwordx4 %0, %1" :: "v"(off), "s"(base), "s"(lds) : "memory")
#define DMAS(off, base, lds)  asm volatile("s_mov_b32 m0, %2\n\ts_nop 0\n\tglobal_load_lds_dwordx4 %0, %1 sc1" :: "v"(off), "s"(base), "s"(lds) : "memory")
#define WAITV(n)              asm volatile("s_waitcnt vmcnt(%0)" :: "i"(n) : "memory")
#define PIN(x)                asm volatile("" : "+v"(x))
#define BAR()                 asm volatile("s_waitcnt lgkmcnt(0)\n\ts_barrier" ::: "memory")
#define BAR_RAW()             asm volatile("s_barrier" ::: "memory")
#define SCHED()               __builtin_amdgcn_sched_barrier(0)

DEV u32x4 tr_frag(const char* p0, const char* p1) {
  u32x2 a = __builtin_bit_cast(u32x2, __builtin_amdgcn_ds_read_tr16_b64_v4i16((LAS s16x4*)p0));
  u32x2 b = __builtin_bit_cast(u32x2, __builtin_amdgcn_ds_read_tr16_b64_v4i16((LAS s16x4*)p1));
  return (u32x4){a[0], a[1], b[0], b[1]};
}

DEV u32x4 trf(u32 a, int o0, int o1) {
  u32x2 x = __builtin_bit_cast(u32x2, __builtin_amdgcn_ds_read_tr16_b64_v4i16((LAS s16x4*)(a + o0)));
  u32x2 y = __builtin_bit_cast(u32x2, __builtin_amdgcn_ds_read_tr16_b64_v4i16((LAS s16x4*)(a + o1)));
  return (u32x4){x[0], x[1], y[0], y[1]};
}

template <int I, int N, class F> DEV void static_for(F&& f) {
  if constexpr (I < N) { f(std::integral_constant<int, I>{}); static_for<I + 1, N>(f); }
}

struct Params { const u16 *x, *lar, *aim, *cr, *ci, *ldt, *dsk, *w1, *w3, *w2, *c, *wm; char* ws; u16* out; };
struct cm { f32x2 b, bp; };
DEV cm mk_cm(float r, float i) { cm m; m.b = (f32x2){r, i}; m.bp = (f32x2){-i, r}; return m; }
DEV f32x2 cmul(f32x2 a, cm m) { f32x2 ax = {a[0], a[0]}, ay = {a[1], a[1]}; return __builtin_elementwise_fma(ay, m.bp, ax * m.b); }
DEV u32 stat_payload(float s1, float s2) {
  const float m = s1 * (1.f / D_);
  const float var = fmaxf(s2 * (1.f / D_) - m * m, 0.f);
  return pack2(m, __builtin_amdgcn_sqrtf(rb(rb(var) + 1e-5f)));
}
DEV void poll_eq14(char* ws, u32 off) {
#pragma nounroll
  for (int sp = 0; sp < SPIN_CAP; sp++) {
    u32 v; asm volatile("global_load_dword %0, %1, %2 sc1\n\ts_waitcnt vmcnt(0)" : "=&v"(v) : "v"(off), "s"(ws) : "memory");
    if (__all(v == 14u)) break;
    __builtin_amdgcn_s_sleep(2);
  }
}
DEV f32x2 up2(u32 u) { return (f32x2){lo_f(u), hi_f(u)}; }
DEV u32 pk2(f32x2 v) { return pack2(v[0], v[1]); }
#define DMA64(addr, lds)  asm volatile("s_mov_b32 m0, %1\n\ts_nop 0\n\tglobal_load_lds_dwordx4 %0, off" :: "v"(addr), "s"(lds) : "memory")
constexpr int rpos(int r) { return (r < 8 ? r : (r ^ 4)) * 272; }
template <int R0> DEV void wrt8(u32 m0v, const u32 (&v)[8]) {
  asm volatile("s_mov_b32 m0, %8\n\ts_nop 0\n\t"
               "ds_write_addtid_b32 %0 offset:%9\n\tds_write_addtid_b32 %1 offset:%10\n\tds_write_addtid_b32 %2 offset:%11\n\tds_write_addtid_b32 %3 offset:%12\n\t"
               "ds_write_addtid_b32 %4 offset:%13\n\tds_write_addtid_b32 %5 offset:%14\n\tds_write_addtid_b32 %6 offset:%15\n\tds_write_addtid_b32 %7 offset:%16"
               :: "v"(v[0]), "v"(v[1]), "v"(v[2]), "v"(v[3]), "v"(v[4]), "v"(v[5]), "v"(v[6]), "v"(v[7]), "s"(m0v),
                  "i"(rpos(R0)), "i"(rpos(R0 + 1)), "i"(rpos(R0 + 2)), "i"(rpos(R0 + 3)), "i"(rpos(R0 + 4)), "i"(rpos(R0 + 5)), "i"(rpos(R0 + 6)), "i"(rpos(R0 + 7)) : "memory");
}
DEV u32 pkh(f32x2 v) { return packh(v[0], v[1]); }
DEV int qidx(int l) {
  u32 bits = pack2((float)l, 0.f) & 0xffffu;
  int q = l < 128 ? l : (int)bits - 0x4300 + 128;
  return l < 0 ? 641 : q;
}

DEV void lds_arrive(char* smem, int idx, int lane) {
  asm volatile("s_waitcnt lgkmcnt(0)" ::: "memory");
  if (lane == 0) __hip_atomic_fetch_add((u32*)(smem + LDS_CNT) + idx, 1u, __ATOMIC_RELAXED, __HIP_MEMORY_SCOPE_WORKGROUP);
}
DEV void lds_wait(char* smem, int idx, u32 n) {
#pragma nounroll
  for (int sp = 0; sp < (1 << 22); sp++) {
    if (__hip_atomic_load((u32*)(smem + LDS_CNT) + idx, __ATOMIC_RELAXED, __HIP_MEMORY_SCOPE_WORKGROUP) >= n) break;
    __builtin_amdgcn_s_sleep(1);
  }
  asm volatile("" ::: "memory");
}

constexpr int pp0_ops(int s) { return ((s & 1) == 0 && s <= 18 ? 1 : 0) + ((s & 1) == 1 && s >= 5 && s <= 25 ? 1 : 0) + (s == 20 ? 5 : 0); }
constexpr int pp0_sum(int a, int b) { int n = 0; for (int s = a; s < b; s++) n += pp0_ops(s); return n; }
constexpr int pp0_wait(int c) { return c <= 3 ? (3 - c) + pp0_sum(0, 4 * c - 2) : pp0_sum(2 * (c - 4) + 1, 4 * c - 2); }
constexpr int LDS_W2 = 114688;

__global__ __launch_bounds__(512, 2) void fused_kernel(Params p) {
  __shared__ __attribute__((aligned(16))) char smem[LDS_TOTAL];
  const int t = threadIdx.x, lane = t & 63;
  const int w = rfl(t >> 6);
  const int b = blockIdx.x, xcd = b & 7, bi = b >> 3;
  const bool is_main = bi < 28;
  char* const ws = p.ws;
  const u32 lbase = (u32)(size_t)smem;
  STAMP_DECL;
  STAMP(0);

  if (OPT_CODEPF && t < 10) {
    const u32 oc = (u32)(bi * 10 + t < CODE_LINES ? bi * 10 + t : 0) * 128, l_ = lbase + LDS_JUNK;
    asm volatile("s_getpc_b64 vcc\n\ts_and_b32 vcc_lo, vcc_lo, 0xffffff80\n\ts_mov_b32 m0, %1\n\ts_nop 0\n\tglobal_load_lds_dword %0, vcc" :: "v"(oc), "s"(l_) : "memory", "vcc", "scc");
  }
  asm volatile("" :: "s"(p.x), "s"(p.w2), "s"(p.ws));
  STAMP(9);
  if (t == 0 && b < 160) { u32 o = OFF_CTR + b * 128; ST4S(o, 0u, ws); }
  if (t < 32) *(u32*)(smem + LDS_CNT + 4 * t) = 0;
  const int row = 8 * b + w;
  const int pid = xcd * 4 + (bi - 28);
  const int pq = pid < 28 ? pid : 0;
  const int wk = w < 7 ? w : 6;
  constexpr int PST = 49152;
  const u32 lst = lbase + PST + w * 14336;
  if (lane < 57) {
    const uint64_t a = lane < 56 ? (uint64_t)p.x + (size_t)row * 896 + lane * 16 : (uint64_t)ws;
    const u32 l_ = lbase + LDS_TX + w * 912;
    asm volatile("s_mov_b32 m0, %1\n\ts_nop 0\n\tglobal_load_lds_dwordx4 %0, off sc1" :: "v"(a), "s"(l_) : "memory");
  }
  const u32 wl_row = lane >> 3, wl_col = ((lane & 7) ^ ((lane >> 2) & 4)) * 16;
  const int mu1 = pid < 21 ? pid : (pid < 28 ? pid + 7 : pid - 7);
  const u32 o_m1 = (u32)(64 * wk + wl_row) * (6 * D_ * 2) + mu1 * 128 + wl_col;
  int bi_early = bi; asm volatile("" : "+s"(bi_early));
  if (bi_early >= 28) {
    { u32 o = (lane < 56 ? lane : 55) * 16, l_ = lst + 12288; DMA(o, p.c, l_); }
  }

  auto get_tag = [&]() { return rfl(*(const u32*)(smem + LDS_TX + w * 912 + 896)) * 1664525u + 1013904223u; };
  auto ln_publish = [&](u32 tag) {
    const int j = 112 * w + (lane < 56 ? lane : 55);
    const u32* src = (const u32*)(smem + LDS_TX) + (j & 3) * 456 + (j >> 2);
    const u32 o = OFF_XG + (u32)(j >> 2) * (L_ * 8) + b * 64 + (j & 3) * 16;
    u32x4 v0 = {src[0], tag, src[228], tag}, v1 = {src[14], tag, src[242], tag};
    u32x4 xr = *(const u32x4*)(smem + LDS_TX + w * 912 + (lane < 56 ? lane : 55) * 16);
    ST16S(o, v0, ws); ST16S(o, v1, ws + 14 * (L_ * 8));
    if (lane >= 56) xr = (u32x4){0, 0, 0, 0};
    float s1 = 0.f, s2 = 0.f, a1 = 0.f, a2 = 0.f;
    s1 = dot2(xr[0], 0x3f803f80u, s1); s2 = dot2(xr[0], xr[0], s2); a1 = dot2(xr[1], 0x3f803f80u, a1); a2 = dot2(xr[1], xr[1], a2);
    s1 = dot2(xr[2], 0x3f803f80u, s1); s2 = dot2(xr[2], xr[2], s2); a1 = dot2(xr[3], 0x3f803f80u, a1); a2 = dot2(xr[3], xr[3], a2);
    float r = red32(s1 + a1, s2 + a2);
    r += dppf<0x111, 0xf>(r); r += dppf<0x112, 0xf>(r); r += dppf<0x114, 0xf>(r); r += dppf<0x118, 0xf>(r); r += dppf<0x142, 0xa>(r);
    const float S = __int_as_float(__builtin_amdgcn_readlane(__float_as_int(r), 31)), Q = __int_as_float(__builtin_amdgcn_readlane(__float_as_int(r), 63));
    u32x2 g = {stat_payload(S, Q), tag};
    if (lane == 0) { u32 oo = OFF_STG + row * 8; ST8S(oo, g, ws); }
  };

  if (__builtin_expect(!is_main, 0)) {
    STAMP(15);
    WAITV(0);
    STAMP(24);
    BAR();
    STAMP(17);
    const u32 tag = get_tag();
    ln_publish(tag);
    STAMP(1);
#pragma unroll
    for (int i = 0; i < 8; i++) { const char* bs = (const char*)p.wm + (size_t)i * 8 * (6 * D_ * 2); u32 l_ = lst + i * 1024; DMA(o_m1, bs, l_); }
    u32* vec = (u32*)(smem);
    float* red = (float*)(smem + 2048);
    if (lane < 32) {
      const u32 craw = *(const u32*)(smem + PST + w * 14336 + 12288 + (32 * wk + lane) * 4);
      float a = lo_f(craw), c2 = hi_f(craw);
      vec[32 * wk + lane] = pack2(a / (1.f + fexp(-a)), c2 / (1.f + fexp(-c2)));
    }
    __builtin_amdgcn_wave_barrier();
    STAMP(16);
    const char* crd0 = smem + PST + w * 14336 + (lane >> 5) * 1024 + ((lane & 15) >> 2) * 128 + ((lane >> 4) & 1) * 32 + (lane & 3) * 8;
    const char* crd[2] = {crd0 + ((lane >> 3) & 1) * 64, crd0 + (((lane >> 3) & 1) ^ 1) * 64};
    u32x4 A[4];
#pragma unroll
    for (int i = 0; i < 4; i++) A[i] = *(const u32x4*)((const char*)vec + (4 * wk + i) * 32 + (lane >> 5) * 16);
    auto mod_unit = [&](int mu, const u32x4 (&Av)[4]) {
      WAITV(0);
#pragma unroll
      for (int h = 0; h < 2; h++) {
        f32x16 a2 = {};
#pragma unroll
        for (int i = 0; i < 4; i++) a2 = __builtin_amdgcn_mfma_f32_32x32x16_bf16(asb(Av[i]), asb(tr_frag(crd[h] + i * 2048, crd[h] + i * 2048 + 512)), a2, 0, 0, 0);
        if (lane < 32) red[w * 256 + h * 32 + lane] = a2[0];
      }
      BAR();
      if (t < 32) {
        f32x2 s = {0.f, 0.f};
#pragma unroll
        for (int i = 0; i < 7; i++) s += *(const f32x2*)(red + i * 256 + 2 * t);
        u32x2 gr = {pk2(s), tag};
        u32 oo = OFF_MODG + (u32)(32 * mu + t) * 8; ST8S(oo, gr, ws);
      }
      BAR();
    };
    const int mu2 = pid < 3 ? 25 + pid : 32 + pid;
    const u32 o_m2 = (u32)(64 * wk + wl_row) * (6 * D_ * 2) + (pid < 10 ? mu2 : 0) * 128 + wl_col;
#pragma nounroll
    for (int rnd = 0; rnd < 2; rnd++) {
      mod_unit(rnd == 0 ? mu1 : mu2, A);
      if (rnd == 0) STAMP(20);
      if (rnd == 1 || pid >= 4) break;
#pragma unroll
      for (int i = 0; i < 8; i++) { const char* bs = (const char*)p.wm + (size_t)i * 8 * (6 * D_ * 2); u32 l_ = lst + i * 1024; DMA(o_m2, bs, l_); }
    }
    STAMP(2);
    if (w < 7 && pid >= 4) {
      const int q = pid - 4;
      const u32 o = (u32)(64 * q + wl_row) * (D_ * 2) + w * 128 + wl_col;
#pragma unroll
      for (int i = 0; i < 8; i++) { const char* bs = (const char*)p.w2 + i * 8 * (D_ * 2); u32 l_ = lst + i * 1024; DMA(o, bs, l_); }
      WAITV(0);
      const u32 oo = OFF_W2T + (u32)((2 * w * 112 + 4 * q) * 64 + lane) * 16;
#pragma unroll
      for (int i = 0; i < 8; i++) { u32x4 f = tr_frag(crd[i >> 2] + (i & 3) * 2048, crd[i >> 2] + (i & 3) * 2048 + 512); const char* bs = ws + ((i >> 2) * 112 + (i & 3)) * 1024; ST16S(oo, f, bs); }
    }
    STAMP(6);
    {
      u32x4 g0, g1; u32x2 gt;
      const u32 og = OFF_STG + w * 2048 + lane * 16, o_gt = OFF_MODG + (u32)(3 * 224 + (t < 448 ? t : 447)) * 8;
      const int cj = (xcd >> 1) * 4 + (bi - 28);
      const int fg = (xcd & 1) * 14 + (cj < 14 ? cj : 0);
      const u32 o = (u32)(64 * wk + wl_row) * (F_ * 2) + fg * 128 + wl_col;
#pragma unroll
      for (int i = 0; i < 8; i++) { const char* bs = (const char*)p.w1 + (size_t)i * 8 * (F_ * 2); u32 l_ = lst + i * 1024; DMA(o, bs, l_); }
#pragma nounroll
      for (int sp = 0; sp < SPIN_CAP; sp++) {
        asm volatile("global_load_dwordx4 %0, %3, %5 sc1\n\tglobal_load_dwordx4 %1, %3, %5 offset:1024 sc1\n\tglobal_load_dwordx2 %2, %4, %5 sc1\n\ts_waitcnt vmcnt(0)"
                     : "=&v"(g0), "=&v"(g1), "=&v"(gt) : "v"(og), "v"(o_gt), "s"(ws) : "memory");
        if (__all(g0[1] == tag && g0[3] == tag && g1[1] == tag && g1[3] == tag && gt[1] == tag)) break;
        __builtin_amdgcn_s_sleep(16);
      }
      if (t < 448) {
        u32 v = gt[0];
        if (t >= 224) v = pack2(1.f + lo_f(v), 1.f + hi_f(v));
        vec[t] = v;
      }
      BAR();
      if (t == 0) { u32 of = OFF_W2F + pid * 4; ST4S(of, tag, ws); }
      u32x4 A2[4];
#pragma unroll
      for (int i = 0; i < 4; i++) A2[i] = *(const u32x4*)((const char*)vec + ((lane & 1) ? 0 : 896) + (4 * wk + i) * 32 + (lane >> 5) * 16);
#pragma unroll
      for (int m = 0; m < 2; m++) {
        u32x4 Bf[8];
#pragma unroll
        for (int i = 0; i < 8; i++) Bf[i] = tr_frag(crd[i >> 2] + (i & 3) * 2048, crd[i >> 2] + (i & 3) * 2048 + 512);
        if (m == 0) {
#pragma unroll
          for (int i = 0; i < 8; i++) PIN(Bf[i]);
#pragma unroll
          for (int i = 0; i < 8; i++) { const char* bs = (const char*)p.w3 + (size_t)i * 8 * (F_ * 2); u32 l_ = lst + i * 1024; DMA(o, bs, l_); }
        }
#pragma unroll
        for (int h = 0; h < 2; h++) {
          f32x16 acc = {};
#pragma unroll
          for (int i = 0; i < 4; i++) acc = __builtin_amdgcn_mfma_f32_32x32x16_bf16(asb(A2[i]), asb(Bf[4 * h + i]), acc, 0, 0, 0);
          if (lane < 32) { red[w * 256 + (2 * m + h) * 64 + lane] = acc[0]; red[w * 256 + (2 * m + h) * 64 + 32 + lane] = acc[1]; }
        }
        if (m == 0) WAITV(0);
      }
      BAR();
      if (t < 256 && cj < 14) {
        float s = 0.f;
#pragma unroll
        for (int i = 0; i < 7; i++) s += red[i * 256 + t];
        u32x2 gr = {__float_as_uint(s), tag};
        u32 oo = OFF_CSG + (u32)(((t >> 5) & 1) * 3584 + (t >> 7) * F_ + fg * 64 + ((t >> 6) & 1) * 32 + (t & 31)) * 8; ST8S(oo, gr, ws);
      }
    }
    STAMP(3);
    if (w == 0) poll_eq14(ws, OFF_CTR + (u32)(xcd * 16 + (lane & 15)) * 128);
    BAR();
    STAMP(4);
    {
      u32x4 v[7];
      u32 o = OFF_X1T + (u32)(t >> 3) * (L_ * 2) + pid * 128 + (t & 7) * 16;
#pragma unroll
      for (int i = 0; i < 7; i++) { const char* bs = ws + (size_t)i * 64 * (L_ * 2); LD16S(v[i], o, bs); }
      asm volatile("s_waitcnt vmcnt(0)" : "+v"(v[0]), "+v"(v[1]), "+v"(v[2]), "+v"(v[3]), "+v"(v[4]), "+v"(v[5]), "+v"(v[6]) :: "memory");
      f32x2 s1[4] = {}, s2[4] = {};
#pragma unroll
      for (int i = 0; i < 7; i++)
#pragma unroll
        for (int q = 0; q < 4; q++) { const f32x2 a = up2(v[i][q]); s1[q] += a; s2[q] = __builtin_elementwise_fma(a, a, s2[q]); }
      float a8[8], a4[4];
#pragma unroll
      for (int q = 0; q < 8; q++) a8[q] = red32(s1[q >> 1][q & 1], s2[q >> 1][q & 1]);
#pragma unroll
      for (int q = 0; q < 4; q++) a4[q] = red16(a8[q], a8[q + 4]);
#pragma unroll
      for (int q = 0; q < 4; q++) a4[q] += dppf<0x128, 0xf>(a4[q]);
      float* rd2 = (float*)smem;
      if (!(lane & 8)) *(f32x4*)(rd2 + (w * 2 + (lane >> 5)) * 64 + (lane & 7) * 8 + ((lane >> 4) & 1) * 4) = (f32x4){a4[0], a4[1], a4[2], a4[3]};
      BAR();
      if (t < 64) {
        float S = 0.f, Q = 0.f;
#pragma unroll
        for (int i = 0; i < 8; i++) { S += rd2[i * 128 + t]; Q += rd2[i * 128 + 64 + t]; }
        u32x2 gr = {stat_payload(S, Q), tag};
        u32 oo = OFF_ST2G + (pid * 64 + t) * 8; ST8S(oo, gr, ws);
      }
    }
    STAMP(5);
    if (pid >= 4 && pid < 10) {
#pragma unroll
      for (int i = 0; i < 8; i++) { const char* bs = (const char*)p.wm + (size_t)i * 8 * (6 * D_ * 2); u32 l_ = lst + i * 1024; DMA(o_m2, bs, l_); }
      BAR();
      mod_unit(mu2, A);
    }
    STAMP_OUT;
    return;
  }

  const int id = xcd * 28 + bi;
  const int pm = (xcd >> 1) * 4 + (bi & 3), cg = (xcd & 1) * 7 + (bi >> 2);
  const int kch = w >> 2, tt = t & 255;
  u32 r_lar, r_aim, r_cr, r_ci, r_ldt, r_dsk;
  {
    u32 o = (u32)((2 * id + kch) * N_ + tt) * 2;
    LD2(r_lar, o, p.lar); LD2(r_aim, o, p.aim); LD2(r_cr, o, p.cr); LD2(r_ci, o, p.ci);
    u32 o2 = (2 * id + kch) * 2; LD2(r_ldt, o2, p.ldt); LD2(r_dsk, o2, p.dsk);
  }
  {
    const u32 q = xcd * 4 + ((lane >> 1) & 3), un = q < 21 ? q : (q < 28 ? q + 7 : q - 7);
    const u32 ot = OPT_TOUCH ? (u32)(16 * bi + 2 * w + (lane & 1)) * (6 * D_ * 2) + un * 128 : 0u, l_ = lbase + LDS_JUNK;
    asm volatile("s_mov_b32 m0, %2\n\ts_nop 0\n\tglobal_load_lds_dword %0, %1" :: "v"(ot), "s"(p.wm), "s"(l_) : "memory");
  }
  STAMP(15);
  const char* const wbase = (const char*)((w & 1) ? p.w3 : p.w1) + (size_t)(8 * (w >> 2)) * (F_ * 2) + (128 * cg + 64 * ((w >> 1) & 1)) * 2;
  const u32 wlane = (u32)(lane >> 3) * (F_ * 2) + ((((lane >> 1) & 3) ^ (2 * (w & 1) + ((lane >> 4) & 1))) * 2 + (lane & 1)) * 16;
  const int krot = OPT_ROT ? (7 * (bi & 3) + 4 * (bi >> 2)) % 28 : 0;
#define KS(s) ((s) + krot >= 28 ? (s) + krot - 28 : (s) + krot)
#define DMA_W2(s, drow, ldsoff) do { const char* a_ = wbase + (int64_t)(16 * KS(s) + (drow)) * (F_ * 2); u32 l_ = lbase + (ldsoff) + w * 1024; DMA(wlane, a_, l_); } while (0)
#define DMA_W(s, ldsoff) DMA_W2(s, 0, ldsoff)
  u32 gix[6];
#pragma unroll
  for (int q = 0; q < 6; q++) { gix[q] = (u32)(2 * qidx(8 * tt - 4 + 2 * q)) | ((u32)(2 * qidx(8 * tt - 3 + 2 * q)) << 16); PIN(gix[q]); }
  STAMP(24);
  WAITV(7);
  BAR();
  STAMP(25);
  const u32 tag = get_tag();
  ln_publish(tag);
  const u32 o_x1 = (u32)(2 * id + kch) * (L_ * 2) + tt * 16;
  STAMP(1);
  asm volatile("s_waitcnt vmcnt(%6)" : "+v"(r_lar), "+v"(r_aim), "+v"(r_cr), "+v"(r_ci), "+v"(r_ldt), "+v"(r_dsk) : "i"(4) : "memory");
  u32x4 W[28];
  const u32 wrd = lbase + (lane >> 5) * 4096 + (w >> 2) * 2048 + ((lane >> 4) & 1) * 1024 + ((lane & 15) >> 2) * 128
                  + ((w & 3) ^ ((lane >> 3) & 3)) * 32 + (((lane & 1) << 1) | ((lane >> 1) & 1)) * 8;
  {
    const float dt = rb(fexp(lo_f(r_ldt)));
    const float a2 = -rb(fexp(lo_f(r_lar))) * dt * 1.44269504088896341f;
    const float bb = lo_f(r_aim) * dt;
    const float bh = bb * 0.15915494309189535f;
    const float bl = __builtin_fmaf(bb, 0.15915494309189535f, -bh) + bb * 6.4206382e-9f;
    auto zpow = [&](float pw) {
      float m = __builtin_amdgcn_exp2f(a2 * pw);
      float th = __builtin_amdgcn_fractf(bh * pw) + bl * pw;
      return (f32x2){m * __builtin_amdgcn_cosf(th), m * __builtin_amdgcn_sinf(th)};
    };
    const f32x2 z1v = zpow(1.f), z8v = zpow(8.f), z128v = zpow(128.f);
    const cm z1 = mk_cm(z1v[0], z1v[1]), z8c = mk_cm(z8v[0], -z8v[1]), z128 = mk_cm(z128v[0], z128v[1]);
    const float c_r = lo_f(r_cr), c_i = lo_f(r_ci);
    const u32 ar_ = r_cr & 0x7fffu, ai_ = r_ci & 0x7fffu;
    int ex = (int)(wave_maxu(ar_ > ai_ ? ar_ : ai_) >> 7) - 126;
    ex = ex < -90 ? -90 : ex;
    if (lane == 0) *(float*)(smem + LDS_CNT + 16 + 4 * w) = __builtin_amdgcn_ldexpf(2.f * dt, ex - 28);
    char* buf = smem + LDS_KG + w * KGW;
    const u32 m0v = lbase + LDS_KG + w * KGW;
    const int c = lane & 15, g = lane >> 4;
    const char* rd = buf + (c < 8 ? c : (c ^ 4)) * 272 + ((g & 1) * 8 + (g >> 1) * 4) * 16;
    f32x2 x[8];
    u32 v[8];
    x[0] = (f32x2){__builtin_amdgcn_ldexpf(c_r, 14 - ex), __builtin_amdgcn_ldexpf(c_i, 14 - ex)};
    f32x2 y = {16384.f, 0.f};
    v[0] = packh(16384.f, 0.f);
#pragma unroll
    for (int i = 1; i < 8; i++) { y = cmul(y, z8c); v[i] = pkh(y); x[i] = cmul(x[i - 1], z1); }
    wrt8<0>(m0v, v);
    u32 v2[8], v3[8];
#pragma unroll
    for (int i = 0; i < 8; i++) { y = cmul(y, z8c); v[i] = pkh(y); v2[i] = pkh(x[i]); x[i] = cmul(x[i], z128); v3[i] = pkh(x[i]); }
    wrt8<8>(m0v, v);
    u32x4 bf[4], af[4];
#pragma unroll
    for (int s = 0; s < 4; s++) bf[s] = *(const u32x4*)(rd + 16 * s);
    wrt8<0>(m0v, v2); wrt8<8>(m0v, v3);
    f32x4 acc[3];
    auto rdA = [&]() {
#pragma unroll
      for (int s = 0; s < 4; s++) af[s] = *(const u32x4*)(rd + 16 * s);
    };
    auto mm = [&](int T) {
      f32x4 a = {0, 0, 0, 0};
#pragma unroll
      for (int s = 0; s < 4; s++) a = __builtin_amdgcn_mfma_f32_16x16x32_f16(ash(af[s]), ash(bf[s]), a, 0, 0, 0);
      acc[T] = a;
    };
    rdA();
#pragma unroll
    for (int r = 0; r < 2; r++)
#pragma unroll
      for (int i = 0; i < 4; i++) { x[2 * i] = cmul(x[2 * i], z128); v[4 * r + i] = pkh(x[2 * i]); }
#pragma unroll
    for (int r = 0; r < 4; r++)
#pragma unroll
      for (int i = 0; i < 2; i++) { x[4 * i] = cmul(x[4 * i], z128); v2[2 * r + i] = pkh(x[4 * i]); }
    if (OPT_TOUCH && w >= 4) {
      const char* tb = w == 4 ? (const char*)p.w1 : w == 5 ? (const char*)p.w3 : w == 6 ? (const char*)p.w2 : (const char*)p.wm;
      const u32 ot = w < 7 ? (u32)(id * 56 + (lane < 56 ? lane : 0)) * 128 : (u32)(2 * id + (lane & 1)) * (6 * D_ * 2) + (((lane >> 1) % 10) < 3 ? 25 + (lane >> 1) % 10 : 32 + (lane >> 1) % 10) * 128;
      const u32 l_ = lbase + LDS_JUNK;
      asm volatile("s_mov_b32 m0, %2\n\ts_nop 0\n\tglobal_load_lds_dword %0, %1" :: "v"(ot), "s"(tb), "s"(l_) : "memory");
    }
    mm(0);
    wrt8<0>(m0v, v); wrt8<8>(m0v, v2);
    rdA();
    {
      const f32x2 q = cmul(z128v, z128);
      const cm z256 = mk_cm(q[0], q[1]);
      f32x2 e = cmul(x[0], z128), o = cmul(x[0], z256);
      v[0] = pkh(e); v[1] = pkh(o);
#pragma unroll
      for (int r = 1; r < 4; r++) { e = cmul(e, z256); o = cmul(o, z256); v[2 * r] = pkh(e); v[2 * r + 1] = pkh(o); }
      e = cmul(e, z256);
      mm(1);
      wrt8<0>(m0v, v);
      *(u32*)(buf + rpos(8) + lane * 4) = pkh(e);
    }
    __builtin_amdgcn_wave_barrier();
    rdA();
    __builtin_amdgcn_wave_barrier();
    mm(2);
    float* tab = (float*)buf;
    *(f32x4*)(tab + 4 * (g & 1) + 128 * (g >> 1) + 8 * c) = acc[0];
    const int q1 = g < 2 ? 256 + 64 * g + 4 * c : 384 + 64 * (g - 2) + 2 * c, q1d = g < 2 ? 2 : 32;
    *(f32x2*)(tab + q1) = (f32x2){acc[1][0], acc[1][1]};
    *(f32x2*)(tab + q1 + q1d) = (f32x2){acc[1][2], acc[1][3]};
#pragma unroll
    for (int r = 0; r < 4; r++) tab[512 + 16 * (4 * g + r) + c] = acc[2][r];
  }
  STAMP(2);
  auto finish_k = [&](auto mid) {
    lds_wait(smem, 18 + kch, 4);
    if (tt < 161) {
      const char* pp = smem + LDS_KG + kch * 4 * KGW + tt * 16;
      const f32x4 sc = *(const f32x4*)(smem + LDS_CNT + 16 + kch * 16);
      f32x4 v = *(const f32x4*)pp * sc[0];
#pragma unroll
      for (int i = 1; i < 4; i++) v = __builtin_elementwise_fma(*(const f32x4*)(pp + i * KGW), (f32x4){sc[i], sc[i], sc[i], sc[i]}, v);
      if (tt == 160) v[1] = 0.f;
      *(u32x2*)(smem + LDS_T16 + kch * 1408 + tt * 8) = (u32x2){pack2(v[0], v[1]), pack2(v[2], v[3])};
    } else if (tt >= 208) {
      const int z = tt - 208;
      const int zo = z < 32 ? LDS_HP + kch * HP_BYTES + ((z >> 4) * 144 + (z & 15)) * 16 : LDS_KC + kch * KC_BYTES + ((z >> 2) & 3) * 4160 + (z & 3) * 16;
      *(u32x4*)(smem + zo) = (u32x4){0, 0, 0, 0};
    }
    lds_arrive(smem, 20 + kch, lane);
    lds_wait(smem, 20 + kch, 4);
    mid();
    const char* t16 = smem + LDS_T16 + kch * 1408;
    u32 wv[6];
#pragma unroll
    for (int q = 0; q < 6; q++) wv[q] = (u32)*(const u16*)(t16 + (gix[q] & 0xffffu)) | ((u32)*(const u16*)(t16 + (gix[q] >> 16)) << 16);
    u32 al[5];
#pragma unroll
    for (int i = 0; i < 5; i++) al[i] = __builtin_amdgcn_alignbit(wv[i + 1], wv[i], 16);
    char* base = smem + LDS_KC + kch * KC_BYTES + 64 + 16 * tt;
    *(u32x4*)(base) = (u32x4){wv[2], wv[3], wv[4], wv[5]};
    *(u32x4*)(base + 4160) = (u32x4){al[1], al[2], al[3], al[4]};
    *(u32x4*)(base + 2 * 4160) = (u32x4){wv[1], wv[2], wv[3], wv[4]};
    *(u32x4*)(base + 3 * 4160) = (u32x4){al[0], al[1], al[2], al[3]};
  };
  lds_arrive(smem, 18 + kch, lane);
  if (w < 4) {
    u32x4 gs[4], gx[4]; u32x2 gm;
    const u32 o_g = OFF_STG + w * 4096 + lane * 16, o_xg = OFF_XG + (u32)id * (L_ * 8) + w * 4096 + lane * 16;
    const u32 o_gm = OFF_MODG + (u32)((lane & 1) * 224 + id) * 8;
    finish_k([&]() {
      asm volatile("global_load_dwordx4 %0, %9, %12 sc1\n\tglobal_load_dwordx4 %1, %9, %12 offset:1024 sc1\n\tglobal_load_dwordx4 %2, %9, %12 offset:2048 sc1\n\tglobal_load_dwordx4 %3, %9, %12 offset:3072 sc1\n\t"
                   "global_load_dwordx4 %4, %10, %12 sc1\n\tglobal_load_dwordx4 %5, %10, %12 offset:1024 sc1\n\tglobal_load_dwordx4 %6, %10, %12 offset:2048 sc1\n\tglobal_load_dwordx4 %7, %10, %12 offset:3072 sc1\n\tglobal_load_dwordx2 %8, %11, %12 sc1"
                   : "=&v"(gs[0]), "=&v"(gs[1]), "=&v"(gs[2]), "=&v"(gs[3]), "=&v"(gx[0]), "=&v"(gx[1]), "=&v"(gx[2]), "=&v"(gx[3]), "=&v"(gm) : "v"(o_g), "v"(o_xg), "v"(o_gm), "s"(ws) : "memory");
    });
    STAMP(16);
    asm volatile("s_waitcnt vmcnt(0)" : "+v"(gs[0]), "+v"(gs[1]), "+v"(gs[2]), "+v"(gs[3]), "+v"(gx[0]), "+v"(gx[1]), "+v"(gx[2]), "+v"(gx[3]), "+v"(gm) :: "memory");
    {
      u32 bad = gm[1] ^ tag;
#pragma unroll
      for (int i = 0; i < 4; i++) bad |= ((gs[i][1] ^ tag) | (gs[i][3] ^ tag)) | ((gx[i][1] ^ tag) | (gx[i][3] ^ tag));
      if (!__all(bad == 0)) {
#pragma nounroll
        for (int sp = 0; sp < SPIN_CAP && !__all(gm[1] == tag); sp++) {
          __builtin_amdgcn_s_sleep(8);
          asm volatile("global_load_dwordx2 %0, %1, %2 sc1\n\ts_waitcnt vmcnt(0)" : "=&v"(gm) : "v"(o_gm), "s"(ws) : "memory");
        }
#pragma nounroll
        for (int sp = 0; sp < SPIN_CAP; sp++) {
          u32 bd = 0;
#pragma unroll
          for (int i = 0; i < 4; i++) bd |= ((gs[i][1] ^ tag) | (gs[i][3] ^ tag)) | ((gx[i][1] ^ tag) | (gx[i][3] ^ tag));
          if (!__any(bd != 0)) break;
          if (sp) __builtin_amdgcn_s_sleep(8);
          if (bd != 0)
            asm volatile("global_load_dwordx4 %0, %8, %10 sc1\n\tglobal_load_dwordx4 %1, %8, %10 offset:1024 sc1\n\tglobal_load_dwordx4 %2, %8, %10 offset:2048 sc1\n\tglobal_load_dwordx4 %3, %8, %10 offset:3072 sc1\n\t"
                         "global_load_dwordx4 %4, %9, %10 sc1\n\tglobal_load_dwordx4 %5, %9, %10 offset:1024 sc1\n\tglobal_load_dwordx4 %6, %9, %10 offset:2048 sc1\n\tglobal_load_dwordx4 %7, %9, %10 offset:3072 sc1\n\ts_waitcnt vmcnt(0)"
                         : "+v"(gs[0]), "+v"(gs[1]), "+v"(gs[2]), "+v"(gs[3]), "+v"(gx[0]), "+v"(gx[1]), "+v"(gx[2]), "+v"(gx[3]) : "v"(o_g), "v"(o_xg), "s"(ws) : "memory");
        }
      }
    }
    lds_arrive(smem, 17, lane);
    STAMP(4);
    const u32 m_sa = __builtin_amdgcn_readlane(gm[0], 0), m_ca = __builtin_amdgcn_readlane(gm[0], 1);
    const f32x2 c1 = up2(pk2(up2(m_ca) + 1.f)), sa = up2(m_sa);
    lds_wait(smem, 19, 4);
    char* hb = smem + LDS_HP + (((lane >> 2) & 1) * 144 + 16 + 32 * w + (lane >> 3)) * 16 + (lane & 3) * 4;
    char* xb_ = smem + LDS_TX + w * 1024 + lane * 4;
#pragma unroll
    for (int i = 0; i < 4; i++) {
      u32 hq[2];
#pragma unroll
      for (int e = 0; e < 2; e++) {
        const u32 pl = gs[i][2 * e];
        const float m = lo_f(pl), rs = __builtin_amdgcn_rcpf(hi_f(pl));
        const f32x2 n = up2(pk2((up2(gx[i][2 * e]) - m) * rs));
        hq[e] = pk2(up2(pk2(n * c1)) + sa);
      }
      *(u32*)(hb + i * 128) = __builtin_amdgcn_perm(hq[1], hq[0], 0x05040100u);
      *(u32*)(hb + i * 128 + HP_BYTES) = __builtin_amdgcn_perm(hq[1], hq[0], 0x07060302u);
      *(u32*)(xb_ + i * 256) = __builtin_amdgcn_perm(gx[i][2], gx[i][0], 0x05040100u);
      *(u32*)(xb_ + i * 256 + 4096) = __builtin_amdgcn_perm(gx[i][2], gx[i][0], 0x07060302u);
    }
  } else {
    finish_k([]() {});
    STAMP(16);
    lds_wait(smem, 17, 4);
#pragma unroll
    for (int s = 0; s < 4; s++) { DMA_W2(s, 0, LDS_S1 + s * 8192); DMA_W2(s, -8, LDS_S1 + s * 8192 - 4096); }
    STAMP(4);
  }
  STAMP(17);
  BAR();
  if (b == 0 && t == 0) { u32 z = 0; ST4S(z, tag, ws); }
  STAMP(5);
  const u32x4 xo_raw = *(const u32x4*)(smem + LDS_TX + kch * 4096 + 16 * tt);
  const u32x4 ho_raw = *(const u32x4*)(smem + LDS_HP + kch * HP_BYTES + ((tt & 1) * 144 + 16 + (tt >> 1)) * 16);
  u32x2 gm2;
  const u32 o_gm2 = OFF_MODG + (u32)(((lane & 1) ? 4 : 2) * 224 + id) * 8;
  LD8S(gm2, o_gm2, ws);
  f32x2 hd[4], xu[4];
  auto pre_epi = [&]() {
    const float dk = lo_f(r_dsk);
#pragma unroll
    for (int q = 0; q < 4; q++) { hd[q] = up2(pk2(up2(ho_raw[q]) * dk)); xu[q] = up2(xo_raw[q]); }
  };
  u32 wrdA = wrd + LDS_S1, wrdB = wrd + LDS_S1 + 5 * 8192;
  PIN(wrdA); PIN(wrdB);
  auto rd_s1 = [&](int s) { return s < 5 ? trf(wrdA, s * 8192, s * 8192 + 512) : trf(wrdB, (s - 5) * 8192, (s - 5) * 8192 + 512); };
  if (w < 4) {
    const int ch = w >> 1, hf = w & 1, r = lane & 15, g = lane >> 4;
    const char* ab = smem + LDS_KC + ch * KC_BYTES + (r >> 2) * 4160 + 2 * (32 + 16 * (1 + 8 * hf) - 8 * g - 4 + 4 * (r & 3));
    const char* bb = smem + LDS_HP + ch * HP_BYTES + ((g & 1) * 144 + 16 + r + (g >> 1) - 7 - 8 * hf) * 16;
    f32x4 acc[8] = {};
    u32x4 A[2][8], B[2][8];
    auto ld = [&](int j, int u) {
      A[u][0] = tr_frag(ab + 64 * j + 8, ab + 64 * j);
#pragma unroll
      for (int d = 7; d >= 4; d--) B[u][d] = *(const u32x4*)(bb + 256 * d + 32 * (3 - j));
#pragma unroll
      for (int a = 1; a < 8; a++) {
        const int o = 512 * a + 64 * j;
        A[u][a] = tr_frag(ab + o + 8, ab + o);
        if (a <= 4) B[u][4 - a] = *(const u32x4*)(bb + 256 * (4 - a) + 32 * (3 - j));
      }
    };
    if (OPT_PRIO) __builtin_amdgcn_s_setprio(2);
    ld(0, 0);
    SCHED();
#pragma unroll
    for (int j = 0; j < 4; j++) {
      const int u = j & 1;
      if (j < 3) ld(j + 1, u ^ 1);
      if (j == 2) {
#pragma unroll
        for (int s = 0; s < 5; s++) W[s] = rd_s1(s);
      }
      if (j == 3) {
#pragma unroll
        for (int s = 5; s < R1; s++) W[s] = rd_s1(s);
        pre_epi();
      }
#pragma unroll
      for (int i = 0; i < 8; i++) {
#pragma unroll
        for (int nt = 7; nt >= 4; nt--)
          if (nt >= i) acc[nt] = __builtin_amdgcn_mfma_f32_16x16x32_bf16(asb(A[u][i]), asb(B[u][nt - i]), acc[nt], 0, 0, 0);
#pragma unroll
        for (int nt = 3; nt >= 0; nt--) {
          const int a = i - 7 + nt;
          if (a >= 0) acc[nt] = __builtin_amdgcn_mfma_f32_16x16x32_bf16(asb(A[u][a]), asb(B[u][nt - a]), acc[nt], 0, 0, 0);
        }
      }
#pragma unroll
      for (int i = 0; i < 36; i++) { __builtin_amdgcn_sched_group_barrier(0x8, 1, 0); __builtin_amdgcn_sched_group_barrier(0x102, 1, 0); }
      if (j == 3) {
#pragma unroll
        for (int q = 0; q < 4; q++) { PIN(hd[q]); PIN(xu[q]); }
      }
      SCHED();
      if (j == 0) { lds_arrive(smem, 16, lane); SCHED(); }
      if (j == 1) { lds_wait(smem, 16, 8); SCHED(); }
      if (j == 2) { lds_arrive(smem, 2, lane); SCHED(); }
    }
    lds_arrive(smem, 3, lane);
    lds_wait(smem, 2, 4);
    const int u0 = 4 * r + g;
    char* yw = smem + LDS_KC + (hf * 2 + ch) * 8192 + (u0 ^ ((u0 >> 3) & 7)) * 16;
#pragma unroll
    for (int nt = 0; nt < 8; nt++) *(f32x4*)(yw + nt * 1024) = acc[nt];
    if (OPT_PRIO) __builtin_amdgcn_s_setprio(0);
  } else {
#pragma unroll
    for (int s = 4; s < R1; s++) { DMA_W2(s, 0, LDS_S1 + s * 8192); DMA_W2(s, -8, LDS_S1 + s * 8192 - 4096); }
    pre_epi();
    if (OPT_PREW) {
      u32x4 z = {0, 0, 0, 0};
      ST16S(o_x1, z, ws + OFF_X1T); ST16S(o_x1, z, ws + OFF_X1S); ST16S(o_x1, z, ws + OFF_X1T - L_ * 2); ST16S(o_x1, z, ws + OFF_X1S - L_ * 2);
    }
    WAITV(OPT_PREW ? 4 : 0);
    lds_arrive(smem, 16, lane);
    lds_wait(smem, 16, 8);
#pragma unroll
    for (int s = 0; s < 5; s++) { DMA_W2(R1 + s, 0, r2slot(s)); DMA_W2(R1 + s, -8, r2slot(s) - 4096); }
#pragma unroll
    for (int s = 0; s < R1; s++) W[s] = rd_s1(s);
    lds_arrive(smem, 3, lane);
  }
  STAMP(6);
  asm volatile("s_waitcnt vmcnt(0)" : "+v"(gm2) :: "memory");
#pragma nounroll
  for (int sp = 0; sp < SPIN_CAP; sp++) {
    if (__all(gm2[1] == tag)) break;
    asm volatile("global_load_dwordx2 %0, %1, %2 sc1\n\ts_waitcnt vmcnt(0)" : "=&v"(gm2) : "v"(o_gm2), "s"(ws) : "memory");
  }
  STAMP(19);
  BAR();
  STAMP(20);
  {
    const char* yr = smem + LDS_KC + kch * 8192;
    const int ua = (2 * tt) ^ ((tt >> 2) & 7);
    const f32x4 ya = *(const f32x4*)(yr + ua * 16) + *(const f32x4*)(yr + 16384 + ua * 16);
    const f32x4 yb = *(const f32x4*)(yr + (ua ^ 1) * 16) + *(const f32x4*)(yr + 16384 + (ua ^ 1) * 16);
    const u32x4 y = {pack2(ya[0], ya[1]), pack2(ya[2], ya[3]), pack2(yb[0], yb[1]), pack2(yb[2], yb[3])};
    const u32 m_ga = __builtin_amdgcn_readlane(gm2[0], 0), m_cb = __builtin_amdgcn_readlane(gm2[0], 1);
    const float ga = kch ? hi_f(m_ga) : lo_f(m_ga), c1b = rb(1.f + (kch ? hi_f(m_cb) : lo_f(m_cb)));
    u32x4 ox, os;
#pragma unroll
    for (int q = 0; q < 4; q++) {
      const f32x2 a = up2(pk2(up2(y[q]) + hd[q]));
      const f32x2 m = up2(pk2(a * ga));
      ox[q] = pk2(xu[q] + m);
      os[q] = pk2(up2(ox[q]) * c1b);
    }
    ST16S(o_x1, os, ws + OFF_X1S); ST16S(o_x1, ox, ws + OFF_X1T);
  }
#pragma unroll
  for (int s = 0; s < 5; s++) W[R1 + s] = trf(wrd, s * 8192, s * 8192 + 512);
  STAMP(21);
  WAITV(0);
  STAMP(22);
  BAR();
  if (t < 8) { u32 o = OFF_CTR + (u32)(t * 16 + (id & 15)) * 128; ATOM_INC(o, ws); }
  STAMP(7);
#pragma unroll
  for (int s = 5; s < R2; s++) DMA_W(R1 + s, r2slot(s));
#pragma unroll
  for (int s = 0; s < R3; s++) DMA_W(R1 + R2 + s, LDS_R3 + s * 8192);
  if (w == 7) {
    const u32 off = lane < 32 ? OFF_CTR + (u32)(xcd * 16 + (lane & 15)) * 128 : OFF_W2F + (lane & 31) * 4;
    const u32 want = lane < 32 ? 14u : tag;
#pragma nounroll
    for (int sp = 0; sp < SPIN_CAP; sp++) {
      u32 v; asm volatile("global_load_dword %0, %1, %2 sc1\n\ts_waitcnt vmcnt(0)" : "=&v"(v) : "v"(off), "s"(ws) : "memory");
      if (__all(v == want)) break;
      __builtin_amdgcn_s_sleep(2);
    }
  }
  WAITV(0);
  BAR();
  STAMP(8);
  const u32 o_xp = OFF_X1S + (u32)(lane >> 3) * (L_ * 2) + (128 * pm) * 2 + ((lane & 7) ^ ((lane >> 2) & 4)) * 16;
  const int xrow0 = 8 * w + 16 * krot;
#define DMA_X(i) do { int r_ = xrow0 + ((i) % 7) * 64; r_ = r_ >= 448 ? r_ - 448 : r_; const char* bs_ = ws + (size_t)r_ * (L_ * 2) + ((i) / 7) * 128; \
    u32 l_ = lbase + (i) * 8192 + w * 1024; DMAS(o_xp, bs_, l_); } while (0)
#pragma unroll
  for (int i = 0; i < 4; i++) DMA_X(i);
  {
    u32 w3 = wrd + LDS_R3; PIN(w3);
#pragma unroll
    for (int s = 5; s < R2; s++) W[R1 + s] = rd_s1(s - 5);
#pragma unroll
    for (int s = 0; s < R3; s++) W[R1 + R2 + s] = trf(w3, s * 8192, s * 8192 + 512);
  }
  u32 xrdA = lbase + (lane >> 5) * 1024 + ((lane & 15) >> 2) * 128 + ((lane >> 4) & 1) * 32 + (lane & 3) * 8 + ((lane >> 3) & 1) * 64;
  u32 xrdB = xrdA ^ 64;
  const int p2 = 2 * xcd + ((bi >> 1) & 1), e2 = bi & 1, dg = bi >> 2;
  const u32 o_w2 = OFF_W2T + (u32)((2 * dg * 112 + 14 * w) * 64 + lane) * 16;
  const int rot = OPT_ROT2 ? 2 * dg : 0;
#define KROT(s) (((s) + rot >= 14 ? (s) + rot - 14 : (s) + rot) * 1024)
  u32x2 st2[4], csg;
  u32x4 Wx, W2[28];
  const u32 o_s2 = OFF_ST2G + (u32)(128 * pm + (lane & 31)) * 8;
  const u32 o_h = OFF_H + (u32)((pm * 4 * 112 + cg * 8 + w) * 64 + lane) * 16;
  const u32 o_cs = OFF_CSG + (u32)((lane >> 5) * 3584 + ((lane >> 4) & 1) * F_ + 128 * cg + 16 * w + ((lane & 3) | ((lane & 8) >> 1) | ((lane & 4) << 1))) * 8;
#define LD_W2(i) do { const char* bs_ = ws + ((i) & 1) * 112 * 1024 + KROT((i) >> 1); LD16S(W2[i], o_w2, bs_); } while (0)

#define LD_ST2(tail) asm volatile("global_load_dwordx2 %0, %5, %7 sc1\n\tglobal_load_dwordx2 %1, %5, %7 offset:256 sc1\n\tglobal_load_dwordx2 %2, %5, %7 offset:512 sc1\n\tglobal_load_dwordx2 %3, %5, %7 offset:768 sc1\n\tglobal_load_dwordx2 %4, %6, %7 sc1" tail \
               : "=&v"(st2[0]), "=&v"(st2[1]), "=&v"(st2[2]), "=&v"(st2[3]), "=&v"(csg) : "v"(o_s2), "v"(o_cs), "s"(ws) : "memory")
  f32x16 a0 = {}, a1 = {}, a2 = {}, a3 = {};
  {
    u32x4 XA[3], XB[3];
    WAITV(3); BAR();
#pragma unroll
    for (int s = 0; s < 2; s++) { XA[s] = trf(xrdA, s * 2048, s * 2048 + 512); XB[s] = trf(xrdB, s * 2048, s * 2048 + 512); }
    static_for<0, 28>([&](auto S) {
      constexpr int s = decltype(S)::value;
      if constexpr (s + 2 < 28) {
        if constexpr (((s + 2) & 3) == 0) { WAITV(pp0_wait((s + 2) / 4)); BAR_RAW(); }
        XA[(s + 2) % 3] = trf(xrdA, (s + 2) * 2048, (s + 2) * 2048 + 512);
        XB[(s + 2) % 3] = trf(xrdB, (s + 2) * 2048, (s + 2) * 2048 + 512);
      }
      a0 = __builtin_amdgcn_mfma_f32_32x32x16_bf16(asb(W[s]), asb(XA[s % 3]), a0, 0, 0, 0);
      a1 = __builtin_amdgcn_mfma_f32_32x32x16_bf16(asb(W[s]), asb(XB[s % 3]), a1, 0, 0, 0);
      PIN(a0); PIN(a1);
      if constexpr ((s & 1) == 0 && s <= 18) DMA_X(4 + s / 2);
      if constexpr ((s & 1) == 1 && s >= 5 && s <= 25) {
        constexpr int j = (s - 5) / 2;
        if constexpr (j < 5) { const char* bs_ = ws + ((23 + j) & 1) * 112 * 1024 + KROT((23 + j) >> 1); u32 l_ = lbase + LDS_W2 + (w * 5 + j) * 1024; DMAS(o_w2, bs_, l_); }
        else LD_W2(j - 5);
      }
      if constexpr (s == 20) LD_ST2("");
      SCHED();
    });
  }
  STAMP(23);
  asm volatile("s_waitcnt vmcnt(3)" : "+v"(st2[0]), "+v"(st2[1]), "+v"(st2[2]), "+v"(st2[3]), "+v"(csg) :: "memory");
  BAR_RAW();
  STAMP(10);
  auto fin = [&](f32x16& acc, int mt) {
    const u32 pl = st2[mt][0];
    const u32 e0 = lane < 32 ? ((pl ^ 0x8000u) & 0xffffu) : (pl >> 16);
    const u32x4 ex = {e0 | (e0 << 16), e0, 0u, 0u};
    acc = __builtin_amdgcn_mfma_f32_32x32x16_bf16(asb(Wx), asb(ex), acc, 0, 0, 0);
  };
  float ec1[4], esq[4];
  f32x2 ee, epr;
  u32x4 ho;
  auto stage = [&](const f32x16& acc, int mt, int j, int st) {
    if (st == 0) {
      const f32x2 gg = {acc[2 * j], acc[2 * j + 1]}, uu = {acc[8 + 2 * j], acc[9 + 2 * j]};
      const f32x2 ar = gg * ec1[mt];
      epr = gg * uu;
      ee = (f32x2){__builtin_amdgcn_exp2f(ar[0]), __builtin_amdgcn_exp2f(ar[1])};
      PIN(ee); PIN(epr);
    } else if (st == 1) {
      const f32x2 den = __builtin_elementwise_fma(ee, (f32x2){esq[mt], esq[mt]}, (f32x2){esq[mt], esq[mt]});
      ee = (f32x2){__builtin_amdgcn_rcpf(den[0]), __builtin_amdgcn_rcpf(den[1])};
      PIN(ee);
    } else {
      u32 v = pk2(epr * ee); PIN(v);
      ho[j] = v;
    }
  };
  auto put = [&](u32x4 o, int mt) { const char* bs = ws + (size_t)mt * (112 * 1024); ST16S(o_h, o, bs); };
  auto signal3 = [&](int half) {
    if (lane == 0) {
      u32* cp = (u32*)(smem + LDS_CNT);
      u32 old = __hip_atomic_fetch_add(cp + half, 1u, __ATOMIC_RELAXED, __HIP_MEMORY_SCOPE_WORKGROUP);
      if (old == 7u) { u32 o = OFF_CTR + (u32)(128 + 2 * pm + half) * 128; ATOM_INC(o, ws); }
    }
  };
  auto wait3 = [&](u32 seen, int half) {
    u32* fl = (u32*)(smem + LDS_CNT + 48) + half;
    const bool ok = __all(seen == 14u);
    if (w == 7) {
      if (!ok) poll_eq14(ws, OFF_CTR + (u32)(128 + 2 * p2 + half) * 128);
      if (lane == 0) __hip_atomic_store(fl, 1u, __ATOMIC_RELAXED, __HIP_MEMORY_SCOPE_WORKGROUP);
    } else if (!ok) {
#pragma nounroll
      for (int sp = 0; sp < (1 << 22); sp++) {
        if (__hip_atomic_load(fl, __ATOMIC_RELAXED, __HIP_MEMORY_SCOPE_WORKGROUP) != 0u) break;
        __builtin_amdgcn_s_sleep(1);
      }
    }
    asm volatile("" ::: "memory");
  };
  xrdA += 57344; xrdB += 57344; PIN(xrdA); PIN(xrdB);
  u32 s3v; bool early;
  const u32 o_s3 = OFF_CTR + (u32)(128 + 2 * p2) * 128;
  u32x4 Hf[14];
  const u32 o_hl = OFF_H + (u32)(((4 * p2 + e2) * 112 + 14 * w) * 64 + lane) * 16;
  {
    u32x4 XA[3], XB[3];
#pragma unroll
    for (int s = 0; s < 2; s++) { XA[s] = trf(xrdA, s * 2048, s * 2048 + 512); XB[s] = trf(xrdB, s * 2048, s * 2048 + 512); }
    auto item = [&](int k) {
      const int n = k / 3, st = k % 3;
      if (n < 4) stage(a0, 0, n, st); else stage(a1, 1, n - 4, st);
      if (st == 2 && (n & 3) == 3) put(ho, n >> 2);
    };
    SCHED();
    static_for<0, 28>([&](auto S) {
      constexpr int s = decltype(S)::value;
      if constexpr (s + 2 < 28) { XA[(s + 2) % 3] = trf(xrdA, (s + 2) * 2048, (s + 2) * 2048 + 512); XB[(s + 2) % 3] = trf(xrdB, (s + 2) * 2048, (s + 2) * 2048 + 512); }
      a2 = __builtin_amdgcn_mfma_f32_32x32x16_bf16(asb(W[s]), asb(XA[s % 3]), a2, 0, 0, 0);
      PIN(a2);
      if constexpr (s >= 2 && s < 14) item(2 * (s - 2));
      SCHED();
      a3 = __builtin_amdgcn_mfma_f32_32x32x16_bf16(asb(W[s]), asb(XB[s % 3]), a3, 0, 0, 0);
      PIN(a3);
      if constexpr (s == 0) {
        if (!__all(st2[0][1] == tag && st2[1][1] == tag && st2[2][1] == tag && st2[3][1] == tag && csg[1] == tag)) {
          WAITV(0);
#pragma nounroll
          for (int sp = 0; sp < SPIN_CAP; sp++) {
            __builtin_amdgcn_s_sleep(4);
            LD_ST2("\n\ts_waitcnt vmcnt(0)");
            if (__all(st2[0][1] == tag && st2[1][1] == tag && st2[2][1] == tag && st2[3][1] == tag && csg[1] == tag)) break;
          }
        }
        const float tot = __uint_as_float(csg[0]);
        const float h0 = rb(tot), h1 = rb(tot - h0), h2 = tot - h0 - h1;
        Wx = (u32x4){pack2(h0, h1), pack2(h2, 0.f), 0u, 0u};
        fin(a0, 0);
      }
      if constexpr (s == 1) {
        fin(a1, 1);
#pragma unroll
        for (int m = 0; m < 4; m++) { const float sd = hi_f(st2[m][0]); ec1[m] = -1.44269504088896341f * __builtin_amdgcn_rcpf(sd); esq[m] = sd * sd; }
      }
      if constexpr (s >= 2 && s < 14) item(2 * (s - 2) + 1);
      if constexpr (s >= 2 && s < 8 && (s & 1) == 0) LD_W2(5 + s / 2);
      if constexpr (s >= 8 && s < 13) LD_W2(s + 1);
      if constexpr (s >= 17 && s < 26) LD_W2(s - 3);
      if constexpr (s == 16) { WAITV(0); signal3(0); }
      if constexpr (s == 27) LD4S(s3v, o_s3, ws);
      SCHED();
    });
    STAMP(28);
    fin(a2, 2); fin(a3, 3);
    asm volatile("s_waitcnt vmcnt(0)" : "+v"(s3v) :: "memory");
#pragma unroll
    for (int i = 0; i < 23; i++) PIN(W2[i]);
    early = __all(s3v == 14u);
    if (early) {
#pragma unroll
      for (int s = 0; s < 4; s++) { const char* bs = ws + KROT(s); u32 l_ = lbase + w * 4096 + s * 1024; DMAS(o_hl, bs, l_); }
    }
#pragma unroll
    for (int m = 2; m < 4; m++) {
      const f32x16 ac = m == 2 ? a2 : a3;
      f32x2 e4[4], p4[4];
#pragma unroll
      for (int j = 0; j < 4; j++) {
        const f32x2 gg = {ac[2 * j], ac[2 * j + 1]}, uu = {ac[8 + 2 * j], ac[9 + 2 * j]};
        const f32x2 ar = gg * ec1[m];
        p4[j] = gg * uu;
        e4[j] = (f32x2){__builtin_amdgcn_exp2f(ar[0]), __builtin_amdgcn_exp2f(ar[1])};
      }
#pragma unroll
      for (int j = 0; j < 4; j++) {
        const f32x2 den = __builtin_elementwise_fma(e4[j], (f32x2){esq[m], esq[m]}, (f32x2){esq[m], esq[m]});
        ho[j] = pk2(p4[j] * (f32x2){__builtin_amdgcn_rcpf(den[0]), __builtin_amdgcn_rcpf(den[1])});
      }
      put(ho, m);
    }
  }
  STAMP(29);
#pragma unroll
  for (int j = 0; j < 5; j++) W2[23 + j] = *(const u32x4*)(smem + LDS_W2 + (w * 5 + j) * 1024 + lane * 16);
  WAITV(0);
  signal3(1);
  wait3(s3v, 0);
  if (!early) {
#pragma unroll
    for (int s = 0; s < 4; s++) { const char* bs = ws + KROT(s); u32 l_ = lbase + w * 4096 + s * 1024; DMAS(o_hl, bs, l_); }
  }
  STAMP(11);
#pragma unroll
  for (int s = 4; s < 11; s++) { const char* bs = ws + KROT(s); LD16S(Hf[s], o_hl, bs); }
  { const char* b1 = ws + 128; LD4S(s3v, o_s3, b1); }
#pragma unroll
  for (int s = 11; s < 14; s++) { const char* bs = ws + KROT(s); LD16S(Hf[s], o_hl, bs); }
  const int ocol = 64 * dg + 32 * (w >> 2) + 8 * (w & 3) + 4 * (lane >> 5);
  const int orow = 128 * p2 + 32 * e2 + (lane & 31);
  u32 xres[8]; u32x4 gbg;
  const u32 o_gb = OFF_MODG + (u32)(5 * 224 + (ocol >> 1)) * 8;
  WAITV(11);
#pragma unroll
  for (int s = 0; s < 4; s++) Hf[s] = *(const u32x4*)(smem + w * 4096 + s * 1024 + lane * 16);
  auto half = [&](auto HH) {
    constexpr int hf = decltype(HH)::value;
    f32x16 f0 = {}, f1 = {};
    static_for<0, 14>([&](auto S) {
      constexpr int s = decltype(S)::value;
      if constexpr (hf == 0 && s >= 4 && s < 11) asm volatile("s_waitcnt vmcnt(%1)" : "+v"(Hf[s]) : "i"(10 - s + 4) : "memory");
      if constexpr (hf == 0 && s == 11) {
        asm volatile("s_waitcnt vmcnt(3)" : "+v"(s3v) :: "memory");
        if (!__all(s3v == 14u)) WAITV(0);
        wait3(s3v, 1);
        STAMP(12);
#pragma unroll
        for (int q = 0; q < 11; q++) { const char* bs = ws + 2 * 112 * 1024 + KROT(q); LD16S(Hf[q], o_hl, bs); }
      }
      if constexpr (hf == 0 && s >= 11) asm volatile("s_waitcnt vmcnt(13)" : "+v"(Hf[s]) :: "memory");
      if constexpr (hf == 1) asm volatile("s_waitcnt vmcnt(%1)" : "+v"(Hf[s]) : "i"(13 - s + 9) : "memory");
      f0 = __builtin_amdgcn_mfma_f32_32x32x16_bf16(asb(W2[2 * s]), asb(Hf[s]), f0, 0, 0, 0);
      f1 = __builtin_amdgcn_mfma_f32_32x32x16_bf16(asb(W2[2 * s + 1]), asb(Hf[s]), f1, 0, 0, 0);
      PIN(f0); PIN(f1);
      if constexpr (hf == 0 && s >= 11) { const char* bs = ws + 2 * 112 * 1024 + KROT(s); LD16S(Hf[s], o_hl, bs); }
      SCHED();
    });
    if constexpr (hf == 0) {
      const u32 o = OFF_X1T + (u32)ocol * (L_ * 2) + orow * 2;
#pragma unroll
      for (int j = 0; j < 8; j++) { const char* bs = ws + (j & 3) * (L_ * 2) + (j >> 2) * 128; LD2S(xres[j], o, bs); }
      LD16S(gbg, o_gb, ws);
      STAMP(30);
    } else {
      asm volatile("s_waitcnt vmcnt(0)" : "+v"(xres[0]), "+v"(xres[1]), "+v"(xres[2]), "+v"(xres[3]), "+v"(xres[4]), "+v"(xres[5]), "+v"(xres[6]), "+v"(xres[7]), "+v"(gbg) :: "memory");
      STAMP(13);
    }
    if (hf == 0) BAR_RAW();
    char* rbuf = smem + hf * 65536;
#pragma unroll
    for (int o = 0; o < 8; o++) {
      const f32x16 sv = (o >> 2) ? f1 : f0; const int i0 = 4 * (o & 3);
      *(f32x4*)(rbuf + (o * 8 + w) * 1024 + lane * 16) = (f32x4){sv[i0], sv[i0 + 1], sv[i0 + 2], sv[i0 + 3]};
    }
    BAR();
    f32x4 sum = *(const f32x4*)(rbuf + (w * 8) * 1024 + lane * 16);
#pragma unroll
    for (int i = 1; i < 8; i++) sum += *(const f32x4*)(rbuf + (w * 8 + i) * 1024 + lane * 16);
    return sum;
  };
  const f32x4 sm0 = half(std::integral_constant<int, 0>{});
  const f32x4 sm1 = half(std::integral_constant<int, 1>{});
#pragma nounroll
  for (int sp = 0; sp < SPIN_CAP; sp++) {
    if (__all(gbg[1] == tag && gbg[3] == tag)) break;
    __builtin_amdgcn_s_sleep(4);
    asm volatile("global_load_dwordx4 %0, %1, %2 sc1\n\ts_waitcnt vmcnt(0)" : "=&v"(gbg) : "v"(o_gb), "s"(ws) : "memory");
  }
#pragma unroll
  for (int hf = 0; hf < 2; hf++) {
    const f32x4 sum = hf ? sm1 : sm0;
    u32x2 res;
#pragma unroll
    for (int j = 0; j < 2; j++) {
      const f32x2 f = up2(pack2(sum[2 * j], sum[2 * j + 1]));
      const f32x2 m = up2(pk2(f * up2(gbg[2 * j])));
      res[j] = pk2((f32x2){lo_f(xres[4 * hf + 2 * j]), lo_f(xres[4 * hf + 2 * j + 1])} + m);
    }
    *(u32x2*)(p.out + (size_t)(orow + 64 * hf) * D_ + ocol) = res;
  }
  STAMP(14);
  STAMP_OUT;
}

extern "C" void kernel_launch(void* const* d_in, const int* in_sizes, int n_in,
                              void* d_out, int out_size, void* d_ws, size_t ws_size,
                              hipStream_t stream) {
  Params p;
  p.x = (const u16*)d_in[0]; p.lar = (const u16*)d_in[1]; p.aim = (const u16*)d_in[2];
  p.cr = (const u16*)d_in[3]; p.ci = (const u16*)d_in[4]; p.ldt = (const u16*)d_in[5]; p.dsk = (const u16*)d_in[6];
  p.w1 = (const u16*)d_in[8]; p.w3 = (const u16*)d_in[9]; p.w2 = (const u16*)d_in[10];
  p.c = (const u16*)d_in[11]; p.wm = (const u16*)d_in[12]; p.ws = (char*)d_ws; p.out = (u16*)d_out;
  fused_kernel<<<256, 512, 0, stream>>>(p);
}
```

```cpp
#include <hip/hip_runtime.h>
#include <stdint.h>
#include <type_traits>

typedef unsigned short u16;
typedef unsigned int u32;
typedef __attribute__((ext_vector_type(8))) __bf16 bf16x8;
typedef __attribute__((ext_vector_type(2))) __bf16 bf16x2;
typedef __attribute__((ext_vector_type(8))) _Float16 f16x8;
typedef __attribute__((ext_vector_type(2))) _Float16 f16x2;
typedef __attribute__((ext_vector_type(4))) short s16x4;
typedef __attribute__((ext_vector_type(2))) float f32x2;
typedef __attribute__((ext_vector_type(4))) float f32x4;
typedef __attribute__((ext_vector_type(16))) float f32x16;
typedef __attribute__((ext_vector_type(4))) u32 u32x4;
typedef __attribute__((ext_vector_type(2))) u32 u32x2;
#define LAS __attribute__((address_space(3)))
#define DEV __device__ __forceinline__

constexpr int L_ = 2048, D_ = 448, N_ = 256, F_ = 1792;
constexpr u32 OFF_E    = 0;
constexpr u32 OFF_CTR  = 4096;
constexpr u32 OFF_W2F  = OFF_CTR + 177 * 128;
constexpr u32 OFF_STG  = 32768;
constexpr u32 OFF_MODG = 49152;
constexpr u32 OFF_ST2G = 65536;
constexpr u32 OFF_CSG  = 81920;
constexpr u32 OFF_XG   = 147456;
constexpr u32 OFF_NG   = OFF_XG + 224 * L_ * 8;
constexpr u32 OFF_X1T  = OFF_NG + 224 * L_ * 8;
constexpr u32 OFF_X1S  = OFF_X1T + D_ * L_ * 2;
constexpr u32 OFF_W2T  = OFF_X1S + D_ * L_ * 2;
constexpr u32 OFF_H    = OFF_W2T + 14 * 112 * 1024;
constexpr u32 OFF_STR  = OFF_H + 64 * 112 * 1024;
constexpr size_t OFF_DBG = (size_t)64 << 20;
constexpr int SPIN_CAP = 1 << 14;

#define OPT_CODEPF 1
#define OPT_TLB 0
#define OPT_ROT 2
#define OPT_ROT2 2
#define OPT_PREW 0
#define OPT_TOUCH 0
#define OPT_PRIO 1
#define OPT_SWEEP_EARLY 0
#define OPT_NOW2 0
#define OPT_PBAR 14
#define OPT_GATE 17
#define PW 7
#define OPT_NG 0
#define NRW 7
#define OPT_LNPOS 1
constexpr int R1 = 9, R2 = 14, R3 = 5;
constexpr int KGW = 4352;
constexpr int LDS_KG  = 0;
constexpr int LDS_T16 = 8 * KGW;
constexpr int LDS_TX  = LDS_T16 + 2816;
constexpr int KC_BYTES = 4 * 4160, HP_BYTES = 2 * 144 * 16;
constexpr int LDS_KC  = LDS_TX + 8192;
constexpr int LDS_HP  = LDS_KC + 2 * KC_BYTES;
constexpr int LDS_S1  = LDS_HP + 2 * HP_BYTES;
constexpr int LDS_CNT = 163840 - 128;
constexpr int LDS_TOTAL = 163840;
constexpr int LDS_R3  = 32768;
constexpr int LDS_JUNK = LDS_CNT - 256;
constexpr int r2slot(int s) { return s < 5 ? s * 8192 : LDS_S1 + (s - 5) * 8192; }
static_assert(5 * 8192 <= LDS_KC && LDS_S1 + 9 * 8192 <= LDS_CNT && LDS_R3 + R3 * 8192 <= LDS_S1 && R1 + R2 + R3 == 28 && (LDS_S1 & 15) == 0, "lds map");
constexpr int CODE_BYTES = 45000;

#define STAMP(i) SCHED()
#define STAMP_DECL do {} while (0)
#define STAMP_OUT do {} while (0)

DEV u32 pack2(float lo, float hi) { f32x2 v = {lo, hi}; bf16x2 b = __builtin_convertvector(v, bf16x2); return __builtin_bit_cast(u32, b); }
DEV u32 packh(float lo, float hi) { f32x2 v = {lo, hi}; f16x2 b = __builtin_convertvector(v, f16x2); return __builtin_bit_cast(u32, b); }
DEV float lo_f(u32 u) { return __uint_as_float(u << 16); }
DEV float hi_f(u32 u) { return __uint_as_float(u & 0xffff0000u); }
DEV float rb(float f) { return lo_f(pack2(f, f)); }
DEV float dot2(u32 a, u32 b, float c) { return __builtin_amdgcn_fdot2_f32_bf16(__builtin_bit_cast(bf16x2, a), __builtin_bit_cast(bf16x2, b), c, false); }
DEV bf16x8 asb(u32x4 v) { return __builtin_bit_cast(bf16x8, v); }
DEV f16x8 ash(u32x4 v) { return __builtin_bit_cast(f16x8, v); }
DEV float fexp(float x) { return __builtin_amdgcn_exp2f(x * 1.44269504088896341f); }
DEV u32 rfl(u32 v) { return __builtin_amdgcn_readfirstlane(v); }

template <int CTRL, int RMASK> DEV float dppf(float v) { return __int_as_float(__builtin_amdgcn_update_dpp(0, __float_as_int(v), CTRL, RMASK, 0xf, false)); }
DEV float wave_sum(float v) {
  v += dppf<0x111, 0xf>(v); v += dppf<0x112, 0xf>(v); v += dppf<0x114, 0xf>(v); v += dppf<0x118, 0xf>(v);
  v += dppf<0x142, 0xa>(v); v += dppf<0x143, 0xc>(v);
  return __int_as_float(__builtin_amdgcn_readlane(__float_as_int(v), 63));
}
template <int CTRL, int RMASK> DEV u32 dppmax(u32 v) { u32 o = (u32)__builtin_amdgcn_update_dpp(0, (int)v, CTRL, RMASK, 0xf, false); return o > v ? o : v; }
DEV u32 wave_maxu(u32 v) {
  v = dppmax<0x111, 0xf>(v); v = dppmax<0x112, 0xf>(v); v = dppmax<0x114, 0xf>(v); v = dppmax<0x118, 0xf>(v);
  v = dppmax<0x142, 0xa>(v); v = dppmax<0x143, 0xc>(v);
  return __builtin_amdgcn_readlane(v, 63);
}
DEV float red32(float a, float b) { auto r = __builtin_amdgcn_permlane32_swap(__float_as_uint(a), __float_as_uint(b), false, false); return __uint_as_float(r[0]) + __uint_as_float(r[1]); }
DEV float red16(float a, float b) { auto r = __builtin_amdgcn_permlane16_swap(__float_as_uint(a), __float_as_uint(b), false, false); return __uint_as_float(r[0]) + __uint_as_float(r[1]); }

#define LD16(d, off, base)    asm volatile("global_load_dwordx4 %0, %1, %2" : "=v"(d) : "v"(off), "s"(base) : "memory")
#define LD16S(d, off, base)   asm volatile("global_load_dwordx4 %0, %1, %2 sc1" : "=v"(d) : "v"(off), "s"(base) : "memory")
#define LD8S(d, off, base)    asm volatile("global_load_dwordx2 %0, %1, %2 sc1" : "=v"(d) : "v"(off), "s"(base) : "memory")
#define LD4(d, off, base)     asm volatile("global_load_dword %0, %1, %2" : "=v"(d) : "v"(off), "s"(base) : "memory")
#define LD4S(d, off, base)    asm volatile("global_load_dword %0, %1, %2 sc1" : "=v"(d) : "v"(off), "s"(base) : "memory")
#define LD2(d, off, base)     asm volatile("global_load_ushort %0, %1, %2" : "=v"(d) : "v"(off), "s"(base) : "memory")
#define LD2S(d, off, base)    asm volatile("global_load_ushort %0, %1, %2 sc1" : "=v"(d) : "v"(off), "s"(base) : "memory")
#define ST16S(off, v, base)   asm volatile("global_store_dwordx4 %0, %1, %2 sc1\n\ts_nop 1" :: "v"(off), "v"(v), "s"(base) : "memory")
#define ST8S(off, v, base)    asm volatile("global_store_dwordx2 %0, %1, %2 sc1" :: "v"(off), "v"(v), "s"(base) : "memory")
#define ST4S(off, v, base)    asm volatile("global_store_dword %0, %1, %2 sc1" :: "v"(off), "v"(v), "s"(base) : "memory")
#define ATOM_INC(off, base)   asm volatile("global_atomic_add %0, %1, %2" :: "v"(off), "v"(1u), "s"(base) : "memory")
#define DMA(off, base, lds)   asm volatile("s_mov_b32 m0, %2\n\ts_nop 0\n\tglobal_load_lds_dwordx4 %0, %1" :: "v"(off), "s"(base), "s"(lds) : "memory")
#define DMAS(off, base, lds)  asm volatile("s_mov_b32 m0, %2\n\ts_nop 0\n\tglobal_load_lds_dwordx4 %0, %1 sc1" :: "v"(off), "s"(base), "s"(lds) : "memory")
#define WAITV(n)              asm volatile("s_waitcnt vmcnt(%0)" :: "i"(n) : "memory")
#define PIN(x)                asm volatile("" : "+v"(x))
#define BAR()                 asm volatile("s_waitcnt lgkmcnt(0)\n\ts_barrier" ::: "memory")
#define BAR_RAW()             asm volatile("s_barrier" ::: "memory")
#define SCHED()               __builtin_amdgcn_sched_barrier(0)

DEV u32x4 tr_frag(const char* p0, const char* p1) {
  u32x2 a = __builtin_bit_cast(u32x2, __builtin_amdgcn_ds_read_tr16_b64_v4i16((LAS s16x4*)p0));
  u32x2 b = __builtin_bit_cast(u32x2, __builtin_amdgcn_ds_read_tr16_b64_v4i16((LAS s16x4*)p1));
  return (u32x4){a[0], a[1], b[0], b[1]};
}

DEV u32x4 trf(u32 a, int o0, int o1) {
  u32x2 x = __builtin_bit_cast(u32x2, __builtin_amdgcn_ds_read_tr16_b64_v4i16((LAS s16x4*)(a + o0)));
  u32x2 y = __builtin_bit_cast(u32x2, __builtin_amdgcn_ds_read_tr16_b64_v4i16((LAS s16x4*)(a + o1)));
  return (u32x4){x[0], x[1], y[0], y[1]};
}

template <int I, int N, class F> DEV void static_for(F&& f) {
  if constexpr (I < N) { f(std::integral_constant<int, I>{}); static_for<I + 1, N>(f); }
}

DEV float smul(float a, float b) { float r; asm("v_mul_f32 %0, %1, %2" : "=v"(r) : "v"(a), "v"(b)); return r; }
DEV float sadd(float a, float b) { float r; asm("v_add_f32 %0, %1, %2" : "=v"(r) : "v"(a), "v"(b)); return r; }
DEV float sfma(float a, float b, float c) { float r; asm("v_fma_f32 %0, %1, %2, %3" : "=v"(r) : "v"(a), "v"(b), "v"(c)); return r; }

struct Params { const u16 *x, *lar, *aim, *cr, *ci, *ldt, *dsk, *w1, *w3, *w2, *c, *wm; char* ws; u16* out; };
struct cm { f32x2 b, bp; };
DEV cm mk_cm(float r, float i) { cm m; m.b = (f32x2){r, i}; m.bp = (f32x2){-i, r}; return m; }
DEV f32x2 cmul(f32x2 a, cm m) { f32x2 ax = {a[0], a[0]}, ay = {a[1], a[1]}; return __builtin_elementwise_fma(ay, m.bp, ax * m.b); }
DEV u32 stat_payload(float s1, float s2) {
  const float m = s1 * (1.f / D_);
  const float var = fmaxf(s2 * (1.f / D_) - m * m, 0.f);
  return pack2(m, __builtin_amdgcn_sqrtf(rb(rb(var) + 1e-5f)));
}
DEV void poll_eq14(char* ws, u32 off) {
#pragma nounroll
  for (int sp = 0; sp < SPIN_CAP; sp++) {
    u32 v; asm volatile("global_load_dword %0, %1, %2 sc1\n\ts_waitcnt vmcnt(0)" : "=&v"(v) : "v"(off), "s"(ws) : "memory");
    if (__all(v == 14u)) break;
    __builtin_amdgcn_s_sleep(2);
  }
}
DEV f32x2 up2(u32 u) { return (f32x2){lo_f(u), hi_f(u)}; }
DEV u32 pk2(f32x2 v) { return pack2(v[0], v[1]); }
#define DMA64(addr, lds)  asm volatile("s_mov_b32 m0, %1\n\ts_nop 0\n\tglobal_load_lds_dwordx4 %0, off" :: "v"(addr), "s"(lds) : "memory")
constexpr int rpos(int r) { return (r < 8 ? r : (r ^ 4)) * 272; }
template <int R0> DEV void wrt8(u32 m0v, const u32 (&v)[8]) {
  asm volatile("s_mov_b32 m0, %8\n\ts_nop 0\n\t"
               "ds_write_addtid_b32 %0 offset:%9\n\tds_write_addtid_b32 %1 offset:%10\n\tds_write_addtid_b32 %2 offset:%11\n\tds_write_addtid_b32 %3 offset:%12\n\t"
               "ds_write_addtid_b32 %4 offset:%13\n\tds_write_addtid_b32 %5 offset:%14\n\tds_write_addtid_b32 %6 offset:%15\n\tds_write_addtid_b32 %7 offset:%16"
               :: "v"(v[0]), "v"(v[1]), "v"(v[2]), "v"(v[3]), "v"(v[4]), "v"(v[5]), "v"(v[6]), "v"(v[7]), "s"(m0v),
                  "i"(rpos(R0)), "i"(rpos(R0 + 1)), "i"(rpos(R0 + 2)), "i"(rpos(R0 + 3)), "i"(rpos(R0 + 4)), "i"(rpos(R0 + 5)), "i"(rpos(R0 + 6)), "i"(rpos(R0 + 7)) : "memory");
}
DEV u32 pkh(f32x2 v) { return packh(v[0], v[1]); }
DEV int qidx(int l) {
  u32 bits = pack2((float)l, 0.f) & 0xffffu;
  int q = l < 128 ? l : (int)bits - 0x4300 + 128;
  return l < 0 ? 641 : q;
}

DEV void lds_arrive(char* smem, int idx, int lane) {
  const u32 a = (u32)(size_t)smem + LDS_CNT + 4 * idx;
  asm volatile("s_waitcnt lgkmcnt(0)\n\ts_mov_b64 exec, 1\n\tds_add_u32 %0, %1\n\ts_mov_b64 exec, -1" :: "v"(a), "v"(1u) : "memory");
}
DEV void lds_post(char* smem, int idx) {
  const u32 a = (u32)(size_t)smem + LDS_CNT + 4 * idx;
  asm volatile("s_mov_b64 exec, 1\n\tds_add_u32 %0, %1\n\ts_mov_b64 exec, -1" :: "v"(a), "v"(1u) : "memory");
}
DEV u32 lds_peek(u32 a) { u32 v; asm volatile("ds_read_b32 %0, %1\n\ts_waitcnt lgkmcnt(0)" : "=v"(v) : "v"(a) : "memory"); return rfl(v); }
DEV void lds_wait(char* smem, int idx, u32 n) {
  const u32 a = (u32)(size_t)smem + LDS_CNT + 4 * idx;
#pragma nounroll
  for (int sp = 0; sp < (1 << 22); sp++) {
    u32 v; asm volatile("ds_read_b32 %0, %1\n\ts_waitcnt lgkmcnt(0)" : "=v"(v) : "v"(a) : "memory");
    if (rfl(v) >= n) break;
    __builtin_amdgcn_s_sleep(1);
  }
}

constexpr int pp0_ops(int s) { return ((s & 1) == 0 && s <= 18 ? 1 : 0) + ((s & 1) == 1 && s >= 5 && s <= 25 ? 1 : 0) + (s == 20 ? 2 : 0); }
constexpr int pp0_sum(int a, int b) { int n = 0; for (int s = a; s < b; s++) n += pp0_ops(s); return n; }
constexpr int pp0_wait(int c) { return c <= 3 ? (3 - c) + pp0_sum(0, 4 * c - 2) : pp0_sum(2 * (c - 4) + 1, 4 * c - 2); }
constexpr int LDS_ST2 = 155648;
constexpr int LDS_W2 = 114688;

__global__ __launch_bounds__(512, 2) void fused_kernel(Params p) {
  __shared__ __attribute__((aligned(16))) char smem[LDS_TOTAL];
  const int t = threadIdx.x, lane = t & 63;
  const int w = rfl(t >> 6);
  const int b = blockIdx.x, xcd = b & 7, bi = b >> 3;
  const bool is_main = bi < 28;
  char* const ws = p.ws;
  const u32 lbase = (u32)(size_t)smem;
  STAMP_DECL;
  STAMP(0);

  asm volatile("" :: "s"(p.x), "s"(p.w2), "s"(p.ws));
  uint64_t pc0; asm volatile("s_getpc_b64 %0" : "=s"(pc0));
  const char* const code0 = (const char*)(pc0 & ~(uint64_t)127);
  STAMP(9);
  if (t < 32 && (t < 4 || t >= 12)) *(u32*)(smem + LDS_CNT + 4 * t) = 0;
  const int row = 8 * b + w;
  const int pid = xcd * 4 + (bi - 28);
  const int pq = pid < 28 ? pid : 0;
  const int wk = w < 7 ? w : 6;
  constexpr int PST = 49152;
  const u32 lst = lbase + PST + w * 14336;
  auto row_dma = [&]() {
  if (lane < 57) {
    const uint64_t a = lane < 56 ? (uint64_t)p.x + (size_t)row * 896 + lane * 16 : (uint64_t)ws;
    const u32 l_ = lbase + LDS_TX + w * 912;
    asm volatile("s_mov_b32 m0, %1\n\ts_nop 0\n\tglobal_load_lds_dwordx4 %0, off sc1" :: "v"(a), "s"(l_) : "memory");
  }
  };
  const u32 wl_row = lane >> 3, wl_col = ((lane & 7) ^ ((lane >> 2) & 4)) * 16;
  const int mu1 = pid < 21 ? pid : (pid < 28 ? pid + 7 : pid - 7);
  const u32 o_m1 = (u32)(64 * wk + wl_row) * (6 * D_ * 2) + mu1 * 128 + wl_col;
  int bi_early = bi; asm volatile("" : "+s"(bi_early));
  if (bi_early >= 28) {
    row_dma();
    { u32 o = (lane < 56 ? lane : 55) * 16, l_ = lst + 12288; DMA(o, p.c, l_); }
  }

  auto get_tag = [&]() { return rfl(*(const u32*)(smem + LDS_TX + w * 912 + 896)) * 1664525u + 1013904223u; };
  auto ln_publish = [&](u32 tag, int tn) {
    u32x4 xr = *(const u32x4*)(smem + LDS_TX + w * 912 + (lane < 56 ? lane : 55) * 16);
    const u32x4 xz = lane < 56 ? xr : (u32x4){0, 0, 0, 0};
    float s1 = 0.f, s2 = 0.f, a1 = 0.f, a2 = 0.f;
    s1 = dot2(xz[0], 0x3f803f80u, s1); s2 = dot2(xz[0], xz[0], s2); a1 = dot2(xz[1], 0x3f803f80u, a1); a2 = dot2(xz[1], xz[1], a2);
    s1 = dot2(xz[2], 0x3f803f80u, s1); s2 = dot2(xz[2], xz[2], s2); a1 = dot2(xz[3], 0x3f803f80u, a1); a2 = dot2(xz[3], xz[3], a2);
    float r = red32(s1 + a1, s2 + a2);
    r += dppf<0x111, 0xf>(r); r += dppf<0x112, 0xf>(r); r += dppf<0x114, 0xf>(r); r += dppf<0x118, 0xf>(r); r += dppf<0x142, 0xa>(r);
    const float S = __int_as_float(__builtin_amdgcn_readlane(__float_as_int(r), 31)), Q = __int_as_float(__builtin_amdgcn_readlane(__float_as_int(r), 63));
    const u32 pay = stat_payload(S, Q);
    const float m = lo_f(pay), rs = __builtin_amdgcn_rcpf(hi_f(pay));
#pragma unroll
    for (int q = 0; q < 4; q++) xr[q] = pk2((up2(xr[q]) - m) * rs);
    *(u32x4*)(smem + tn + w * 912 + (lane < 56 ? lane : 56) * 16) = xr;
    if (w == 0) WAITV(1);
    BAR();
    const int j = 112 * w + (lane < 56 ? lane : 55);
    const u32* src = (const u32*)(smem + LDS_TX) + (j & 3) * 456 + (j >> 2);
    const u32* srn = (const u32*)(smem + tn) + (j & 3) * 456 + (j >> 2);
    const u32 o = (u32)(j >> 2) * (L_ * 8) + b * 64 + (j & 3) * 16;
    u32x4 n0 = {srn[0], tag, srn[228], tag}, n1 = {srn[14], tag, srn[242], tag};
    u32x4 v0 = {src[0], tag, src[228], tag}, v1 = {src[14], tag, src[242], tag};
    ST16S(o, n0, ws + OFF_NG); ST16S(o, n1, ws + OFF_NG + 14 * (L_ * 8));
    ST16S(o, v0, ws + OFF_XG); ST16S(o, v1, ws + OFF_XG + 14 * (L_ * 8));
    if (t == 0) { u32 oo = OFF_STG + b * 64; ST4S(oo, tag, ws); }
  };

  auto ln_publish0 = [&](u32 tag) {
    BAR();
    const int j = 112 * w + (lane < 56 ? lane : 55);
    const u32* src = (const u32*)(smem + LDS_TX) + (j & 3) * 456 + (j >> 2);
    const u32 o = OFF_XG + (u32)(j >> 2) * (L_ * 8) + b * 64 + (j & 3) * 16;
    u32x4 v0 = {src[0], tag, src[228], tag}, v1 = {src[14], tag, src[242], tag};
    u32x4 xr = *(const u32x4*)(smem + LDS_TX + w * 912 + (lane < 56 ? lane : 55) * 16);
    ST16S(o, v0, ws); ST16S(o, v1, ws + 14 * (L_ * 8));
    if (lane >= 56) xr = (u32x4){0, 0, 0, 0};
    float s1 = 0.f, s2 = 0.f, a1 = 0.f, a2 = 0.f;
    s1 = dot2(xr[0], 0x3f803f80u, s1); s2 = dot2(xr[0], xr[0], s2); a1 = dot2(xr[1], 0x3f803f80u, a1); a2 = dot2(xr[1], xr[1], a2);
    s1 = dot2(xr[2], 0x3f803f80u, s1); s2 = dot2(xr[2], xr[2], s2); a1 = dot2(xr[3], 0x3f803f80u, a1); a2 = dot2(xr[3], xr[3], a2);
    float r = red32(s1 + a1, s2 + a2);
    r += dppf<0x111, 0xf>(r); r += dppf<0x112, 0xf>(r); r += dppf<0x114, 0xf>(r); r += dppf<0x118, 0xf>(r); r += dppf<0x142, 0xa>(r);
    const float S = __int_as_float(__builtin_amdgcn_readlane(__float_as_int(r), 31)), Q = __int_as_float(__builtin_amdgcn_readlane(__float_as_int(r), 63));
    const u32 pay = stat_payload(S, Q);
    if (w == 0) WAITV(2);
    u32 old;
    { const u32 a = lbase + LDS_CNT + 96 + 4 * w, c = lbase + LDS_CNT + 4 * 0;
      asm volatile("s_mov_b64 exec, 1\n\tds_write_b32 %1, %2\n\tds_add_rtn_u32 %0, %3, %4\n\ts_mov_b64 exec, -1\n\ts_waitcnt lgkmcnt(0)" : "=&v"(old) : "v"(a), "v"(pay), "v"(c), "v"(1u) : "memory"); }
    if (rfl(old) == 7u) {
      u32x2 g = {*(const u32*)(smem + LDS_CNT + 96 + 4 * (lane & 7)), tag};
      u32 oo = OFF_STR + (lane >> 3) * 16384 + (8 * b + (lane & 7)) * 8; ST8S(oo, g, ws);
    }
  };

  if (__builtin_expect(!is_main, 0)) {
    STAMP(15);
    WAITV(0);
    STAMP(24);
    STAMP(17);
    const u32 tag = get_tag();
    if (OPT_NG) ln_publish(tag, 16384); else ln_publish0(tag);
    STAMP(1);
#pragma unroll
    for (int i = 0; i < 8; i++) { const char* bs = (const char*)p.wm + (size_t)i * 8 * (6 * D_ * 2); u32 l_ = lst + i * 1024; DMA(o_m1, bs, l_); }
    u32* vec = (u32*)(smem);
    float* red = (float*)(smem + 2048);
    if (lane < 32) {
      const u32 craw = *(const u32*)(smem + PST + w * 14336 + 12288 + (32 * wk + lane) * 4);
      float a = lo_f(craw), c2 = hi_f(craw);
      vec[32 * wk + lane] = pack2(a / (1.f + fexp(-a)), c2 / (1.f + fexp(-c2)));
    }
    __builtin_amdgcn_wave_barrier();
    STAMP(16);
    const char* crd0 = smem + PST + w * 14336 + (lane >> 5) * 1024 + ((lane & 15) >> 2) * 128 + ((lane >> 4) & 1) * 32 + (lane & 3) * 8;
    const char* crd[2] = {crd0 + ((lane >> 3) & 1) * 64, crd0 + (((lane >> 3) & 1) ^ 1) * 64};
    u32x4 A[4];
#pragma unroll
    for (int i = 0; i < 4; i++) A[i] = *(const u32x4*)((const char*)vec + (4 * wk + i) * 32 + (lane >> 5) * 16);
    auto mod_unit = [&](int mu, const u32x4 (&Av)[4]) {
      WAITV(0);
#pragma unroll
      for (int h = 0; h < 2; h++) {
        f32x16 a2 = {};
#pragma unroll
        for (int i = 0; i < 4; i++) a2 = __builtin_amdgcn_mfma_f32_32x32x16_bf16(asb(Av[i]), asb(tr_frag(crd[h] + i * 2048, crd[h] + i * 2048 + 512)), a2, 0, 0, 0);
        if (lane < 32) red[w * 256 + h * 32 + lane] = a2[0];
      }
      BAR();
      if (t < 32) {
        f32x2 s = {0.f, 0.f};
#pragma unroll
        for (int i = 0; i < 7; i++) s += *(const f32x2*)(red + i * 256 + 2 * t);
        u32x2 gr = {pk2(s), tag};
        u32 oo = OFF_MODG + (u32)(32 * mu + t) * 8; ST8S(oo, gr, ws);
      }
      BAR();
    };
    const int mu2 = pid < 3 ? 25 + pid : 32 + pid;
    const u32 o_m2 = (u32)(64 * wk + wl_row) * (6 * D_ * 2) + (pid < 10 ? mu2 : 0) * 128 + wl_col;
#pragma nounroll
    for (int rnd = 0; rnd < 2; rnd++) {
      mod_unit(rnd == 0 ? mu1 : mu2, A);
      if (rnd == 0) STAMP(20);
      if (rnd == 1 || pid >= 4) break;
#pragma unroll
      for (int i = 0; i < 8; i++) { const char* bs = (const char*)p.wm + (size_t)i * 8 * (6 * D_ * 2); u32 l_ = lst + i * 1024; DMA(o_m2, bs, l_); }
    }
    STAMP(2);
    if (w < 7 && pid >= 4) {
      const int q = pid - 4;
      const u32 o = (u32)(64 * q + wl_row) * (D_ * 2) + w * 128 + wl_col;
#pragma unroll
      for (int i = 0; i < 8; i++) { const char* bs = (const char*)p.w2 + i * 8 * (D_ * 2); u32 l_ = lst + i * 1024; DMA(o, bs, l_); }
      WAITV(0);
      const u32 oo = OFF_W2T + (u32)((2 * w * 112 + 4 * q) * 64 + lane) * 16;
#pragma unroll
      for (int i = 0; i < 8; i++) { u32x4 f = tr_frag(crd[i >> 2] + (i & 3) * 2048, crd[i >> 2] + (i & 3) * 2048 + 512); const char* bs = ws + ((i >> 2) * 112 + (i & 3)) * 1024; ST16S(oo, f, bs); }
    }
    {
      u32 g0; const u32 og = (u32)(32 * w + (lane & 31)) * 64 + (OPT_NG ? OFF_STG : OFF_STR + xcd * 16384 + 4);
#pragma nounroll
      for (int sp = 0; sp < SPIN_CAP; sp++) {
        asm volatile("global_load_dword %0, %1, %2 sc1\n\ts_waitcnt vmcnt(0)" : "=&v"(g0) : "v"(og), "s"(ws) : "memory");
        if (__all(g0 == tag)) break;
        __builtin_amdgcn_s_sleep(16);
      }
    }
    BAR();
    if (w == 7) {
      u32 old = 0;
      if (lane == 0) old = __hip_atomic_fetch_add((u32*)(ws + OFF_CTR + 176 * 128), 1u, __ATOMIC_RELAXED, __HIP_MEMORY_SCOPE_AGENT);
      if (rfl(old) == 31u && lane < 8) { u32 of = OFF_W2F + lane * 128; ST4S(of, tag, ws); }
    }
    STAMP(6);
    {
      u32x2 gt;
      const u32 o_gt = OFF_MODG + (u32)(3 * 224 + (t < 448 ? t : 447)) * 8;
      const int cj = (xcd >> 1) * 4 + (bi - 28);
      const int fg = (xcd & 1) * 14 + (cj < 14 ? cj : 0);
      const u32 o = (u32)(64 * wk + wl_row) * (F_ * 2) + fg * 128 + wl_col;
#pragma unroll
      for (int i = 0; i < 8; i++) { const char* bs = (const char*)p.w1 + (size_t)i * 8 * (F_ * 2); u32 l_ = lst + i * 1024; DMA(o, bs, l_); }
#pragma nounroll
      for (int sp = 0; sp < SPIN_CAP; sp++) {
        asm volatile("global_load_dwordx2 %0, %1, %2 sc1\n\ts_waitcnt vmcnt(0)" : "=&v"(gt) : "v"(o_gt), "s"(ws) : "memory");
        if (__all(gt[1] == tag)) break;
        __builtin_amdgcn_s_sleep(16);
      }
      if (t < 448) {
        u32 v = gt[0];
        if (t >= 224) v = pack2(1.f + lo_f(v), 1.f + hi_f(v));
        vec[t] = v;
      }
      BAR();
      u32x4 A2[4];
#pragma unroll
      for (int i = 0; i < 4; i++) A2[i] = *(const u32x4*)((const char*)vec + ((lane & 1) ? 0 : 896) + (4 * wk + i) * 32 + (lane >> 5) * 16);
#pragma unroll
      for (int m = 0; m < 2; m++) {
        u32x4 Bf[8];
#pragma unroll
        for (int i = 0; i < 8; i++) Bf[i] = tr_frag(crd[i >> 2] + (i & 3) * 2048, crd[i >> 2] + (i & 3) * 2048 + 512);
        if (m == 0) {
#pragma unroll
          for (int i = 0; i < 8; i++) PIN(Bf[i]);
#pragma unroll
          for (int i = 0; i < 8; i++) { const char* bs = (const char*)p.w3 + (size_t)i * 8 * (F_ * 2); u32 l_ = lst + i * 1024; DMA(o, bs, l_); }
        }
#pragma unroll
        for (int h = 0; h < 2; h++) {
          f32x16 acc = {};
#pragma unroll
          for (int i = 0; i < 4; i++) acc = __builtin_amdgcn_mfma_f32_32x32x16_bf16(asb(A2[i]), asb(Bf[4 * h + i]), acc, 0, 0, 0);
          if (lane < 32) { red[w * 256 + (2 * m + h) * 64 + lane] = acc[0]; red[w * 256 + (2 * m + h) * 64 + 32 + lane] = acc[1]; }
        }
        if (m == 0) WAITV(0);
      }
      BAR();
      if (t < 256 && cj < 14) {
        float s = 0.f;
#pragma unroll
        for (int i = 0; i < 7; i++) s += red[i * 256 + t];
        u32x2 gr = {__float_as_uint(s), tag};
        u32 oo = OFF_CSG + (u32)(((t >> 5) & 1) * 3584 + (t >> 7) * F_ + fg * 64 + ((t >> 6) & 1) * 32 + (t & 31)) * 8; ST8S(oo, gr, ws);
      }
    }
    STAMP(3);
    if (w == 0) poll_eq14(ws, OFF_CTR + (u32)(xcd * 16 + (lane & 15)) * 128);
    BAR();
    STAMP(4);
    {
      u32x4 v[7];
      u32 o = OFF_X1T + (u32)(t >> 3) * (L_ * 2) + pid * 128 + (t & 7) * 16;
#pragma unroll
      for (int i = 0; i < 7; i++) { const char* bs = ws + (size_t)i * 64 * (L_ * 2); LD16S(v[i], o, bs); }
      asm volatile("s_waitcnt vmcnt(0)" : "+v"(v[0]), "+v"(v[1]), "+v"(v[2]), "+v"(v[3]), "+v"(v[4]), "+v"(v[5]), "+v"(v[6]) :: "memory");
      f32x2 s1[4] = {}, s2[4] = {};
#pragma unroll
      for (int i = 0; i < 7; i++)
#pragma unroll
        for (int q = 0; q < 4; q++) { const f32x2 a = up2(v[i][q]); s1[q] += a; s2[q] = __builtin_elementwise_fma(a, a, s2[q]); }
      float a8[8], a4[4];
#pragma unroll
      for (int q = 0; q < 8; q++) a8[q] = red32(s1[q >> 1][q & 1], s2[q >> 1][q & 1]);
#pragma unroll
      for (int q = 0; q < 4; q++) a4[q] = red16(a8[q], a8[q + 4]);
#pragma unroll
      for (int q = 0; q < 4; q++) a4[q] += dppf<0x128, 0xf>(a4[q]);
      float* rd2 = (float*)smem;
      if (!(lane & 8)) *(f32x4*)(rd2 + (w * 2 + (lane >> 5)) * 64 + (lane & 7) * 8 + ((lane >> 4) & 1) * 4) = (f32x4){a4[0], a4[1], a4[2], a4[3]};
      BAR();
      if (t < 64) {
        float S = 0.f, Q = 0.f;
#pragma unroll
        for (int i = 0; i < 8; i++) { S += rd2[i * 128 + t]; Q += rd2[i * 128 + 64 + t]; }
        u32x2 gr = {stat_payload(S, Q), tag};
        u32 oo = OFF_ST2G + (pid * 64 + t) * 8; ST8S(oo, gr, ws);
      }
    }
    STAMP(5);
    if (pid >= 4 && pid < 10) {
#pragma unroll
      for (int i = 0; i < 8; i++) { const char* bs = (const char*)p.wm + (size_t)i * 8 * (6 * D_ * 2); u32 l_ = lst + i * 1024; DMA(o_m2, bs, l_); }
      BAR();
      mod_unit(mu2, A);
    }
    STAMP_OUT;
    return;
  }

  const int id = xcd * 28 + bi;
  const int pm = (xcd >> 1) * 4 + (bi & 3), cg = (xcd & 1) * 7 + (bi >> 2);
  const int kch = w >> 2, tt = t & 255;
  u32 r_lar, r_aim, r_cr, r_ci, r_ldt;
  {
    u32 o = (u32)((2 * id + kch) * N_ + tt) * 2;
    LD2(r_lar, o, p.lar); LD2(r_aim, o, p.aim); LD2(r_cr, o, p.cr); LD2(r_ci, o, p.ci);
    u32 o2 = (2 * id + kch) * 2; LD2(r_ldt, o2, p.ldt);
  }
  row_dma();
  {
    u32 oc = (u32)(2 * (8 * bi + w) + (lane & 1)) * 128; oc = (OPT_CODEPF && oc < CODE_BYTES) ? oc : 0u;
    const u32 l_ = lbase + LDS_JUNK;
    asm volatile("s_mov_b32 m0, %2\n\ts_nop 0\n\tglobal_load_lds_dword %0, %1" :: "v"(oc), "s"(code0), "s"(l_) : "memory");
  }
  if (t == 0) { u32 o = OFF_CTR + b * 128; ST4S(o, 0u, ws); }
  STAMP(15);
  const char* const wbase = (const char*)((w & 1) ? p.w3 : p.w1) + (size_t)(8 * (w >> 2)) * (F_ * 2) + (128 * cg + 64 * ((w >> 1) & 1)) * 2;
  const u32 wlane = (u32)(lane >> 3) * (F_ * 2) + ((((lane >> 1) & 3) ^ (2 * (w & 1) + ((lane >> 4) & 1))) * 2 + (lane & 1)) * 16;
  const int krot = OPT_ROT == 1 ? (7 * (bi & 3) + 4 * (bi >> 2)) % 28 : (OPT_ROT == 2 ? 7 * (xcd >> 1) : 0);
#define KS(s) ((s) + krot >= 28 ? (s) + krot - 28 : (s) + krot)
#define DMA_W2(s, drow, ldsoff) do { const char* a_ = wbase + (int64_t)(16 * KS(s) + (drow)) * (F_ * 2); u32 l_ = lbase + (ldsoff) + w * 1024; DMA(wlane, a_, l_); } while (0)
#define DMA_W(s, ldsoff) DMA_W2(s, 0, ldsoff)
  u32 gix[6];
#pragma unroll
  for (int q = 0; q < 6; q++) { gix[q] = (u32)(2 * qidx(8 * tt - 4 + 2 * q)) | ((u32)(2 * qidx(8 * tt - 3 + 2 * q)) << 16); PIN(gix[q]); }
  STAMP(24);
  u32 tag;
  auto do_ln = [&]() {
    if (w == 0) WAITV(2); else WAITV(1);
    STAMP(25);
    tag = get_tag();
    ln_publish0(tag);
    lds_post(smem, 14);
    STAMP(1);
  };
  if (OPT_LNPOS == 0) do_ln();
  if (w != 0) WAITV(OPT_LNPOS ? 2 : 4);
  asm volatile("s_waitcnt vmcnt(%5)" : "+v"(r_lar), "+v"(r_aim), "+v"(r_cr), "+v"(r_ci), "+v"(r_ldt) : "i"(OPT_LNPOS ? 3 : 4) : "memory");
  u32x4 W[28];
  const u32 wrd = lbase + (lane >> 5) * 4096 + (w >> 2) * 2048 + ((lane >> 4) & 1) * 1024 + ((lane & 15) >> 2) * 128
                  + ((w & 3) ^ ((lane >> 3) & 3)) * 32 + (((lane & 1) << 1) | ((lane >> 1) & 1)) * 8;
  {
    const float dt = rb(fexp(lo_f(r_ldt)));
    const float a2 = -rb(fexp(lo_f(r_lar))) * dt * 1.44269504088896341f;
    const float bb = lo_f(r_aim) * dt;
    const float bh = bb * 0.15915494309189535f;
    const float bl = __builtin_fmaf(bb, 0.15915494309189535f, -bh) + bb * 6.4206382e-9f;
    auto zpow = [&](float pw) {
      float m = __builtin_amdgcn_exp2f(a2 * pw);
      float th = __builtin_amdgcn_fractf(bh * pw) + bl * pw;
      return (f32x2){m * __builtin_amdgcn_cosf(th), m * __builtin_amdgcn_sinf(th)};
    };
    const f32x2 z1v = zpow(1.f), z8v = zpow(8.f), z128v = zpow(128.f);
    const cm z1 = mk_cm(z1v[0], z1v[1]), z8c = mk_cm(z8v[0], -z8v[1]), z128 = mk_cm(z128v[0], z128v[1]);
    const float c_r = lo_f(r_cr), c_i = lo_f(r_ci);
    const u32 ar_ = r_cr & 0x7fffu, ai_ = r_ci & 0x7fffu;
    int ex = (int)(wave_maxu(ar_ > ai_ ? ar_ : ai_) >> 7) - 126;
    ex = ex < -90 ? -90 : ex;
    if (lane == 0) *(float*)(smem + LDS_CNT + 16 + 4 * w) = __builtin_amdgcn_ldexpf(2.f * dt, ex - 28);
    if (OPT_LNPOS == 1) do_ln();
    char* buf = smem + LDS_KG + w * KGW;
    const u32 m0v = lbase + LDS_KG + w * KGW;
    const int c = lane & 15, g = lane >> 4;
    const char* rd = buf + (c < 8 ? c : (c ^ 4)) * 272 + ((g & 1) * 8 + (g >> 1) * 4) * 16;
    f32x2 x[8];
    u32 v[8];
    x[0] = (f32x2){__builtin_amdgcn_ldexpf(c_r, 14 - ex), __builtin_amdgcn_ldexpf(c_i, 14 - ex)};
    f32x2 y = {16384.f, 0.f};
    v[0] = packh(16384.f, 0.f);
#pragma unroll
    for (int i = 1; i < 8; i++) { y = cmul(y, z8c); v[i] = pkh(y); x[i] = cmul(x[i - 1], z1); }
    wrt8<0>(m0v, v);
    if (OPT_LNPOS == 2) do_ln();
    u32 v2[8], v3[8];
#pragma unroll
    for (int i = 0; i < 8; i++) { y = cmul(y, z8c); v[i] = pkh(y); v2[i] = pkh(x[i]); x[i] = cmul(x[i], z128); v3[i] = pkh(x[i]); }
    wrt8<8>(m0v, v);
    u32x4 bf[4], af[4];
#pragma unroll
    for (int s = 0; s < 4; s++) bf[s] = *(const u32x4*)(rd + 16 * s);
    wrt8<0>(m0v, v2); wrt8<8>(m0v, v3);
    if (OPT_LNPOS == 3) do_ln();
    f32x4 acc[3];
    auto rdA = [&]() {
#pragma unroll
      for (int s = 0; s < 4; s++) af[s] = *(const u32x4*)(rd + 16 * s);
    };
    auto mm = [&](int T) {
      f32x4 a = {0, 0, 0, 0};
#pragma unroll
      for (int s = 0; s < 4; s++) a = __builtin_amdgcn_mfma_f32_16x16x32_f16(ash(af[s]), ash(bf[s]), a, 0, 0, 0);
      acc[T] = a;
    };
    rdA();
#pragma unroll
    for (int r = 0; r < 2; r++)
#pragma unroll
      for (int i = 0; i < 4; i++) { x[2 * i] = cmul(x[2 * i], z128); v[4 * r + i] = pkh(x[2 * i]); }
#pragma unroll
    for (int r = 0; r < 4; r++)
#pragma unroll
      for (int i = 0; i < 2; i++) { x[4 * i] = cmul(x[4 * i], z128); v2[2 * r + i] = pkh(x[4 * i]); }
    mm(0);
    wrt8<0>(m0v, v); wrt8<8>(m0v, v2);
    rdA();
    {
      const f32x2 q = cmul(z128v, z128);
      const cm z256 = mk_cm(q[0], q[1]);
      f32x2 e = cmul(x[0], z128), o = cmul(x[0], z256);
      v[0] = pkh(e); v[1] = pkh(o);
#pragma unroll
      for (int r = 1; r < 4; r++) { e = cmul(e, z256); o = cmul(o, z256); v[2 * r] = pkh(e); v[2 * r + 1] = pkh(o); }
      e = cmul(e, z256);
      mm(1);
      wrt8<0>(m0v, v);
      *(u32*)(buf + rpos(8) + lane * 4) = pkh(e);
    }
    __builtin_amdgcn_wave_barrier();
    rdA();
    __builtin_amdgcn_wave_barrier();
    mm(2);
    float* tab = (float*)buf;
    *(f32x4*)(tab + 4 * (g & 1) + 128 * (g >> 1) + 8 * c) = acc[0];
    const int q1 = g < 2 ? 256 + 64 * g + 4 * c : 384 + 64 * (g - 2) + 2 * c, q1d = g < 2 ? 2 : 32;
    *(f32x2*)(tab + q1) = (f32x2){acc[1][0], acc[1][1]};
    *(f32x2*)(tab + q1 + q1d) = (f32x2){acc[1][2], acc[1][3]};
#pragma unroll
    for (int r = 0; r < 4; r++) tab[512 + 16 * (4 * g + r) + c] = acc[2][r];
  }
  STAMP(2);
  auto finish_k = [&](auto mid) {
    lds_wait(smem, 18 + kch, 4);
    if (OPT_NG) lds_wait(smem, 14, 8);
    if (tt < 161) {
      const char* pp = smem + LDS_KG + kch * 4 * KGW + tt * 16;
      const f32x4 sc = *(const f32x4*)(smem + LDS_CNT + 16 + kch * 16);
      f32x4 v = *(const f32x4*)pp * sc[0];
#pragma unroll
      for (int i = 1; i < 4; i++) v = __builtin_elementwise_fma(*(const f32x4*)(pp + i * KGW), (f32x4){sc[i], sc[i], sc[i], sc[i]}, v);
      if (tt == 160) v[1] = 0.f;
      *(u32x2*)(smem + LDS_T16 + kch * 1408 + tt * 8) = (u32x2){pack2(v[0], v[1]), pack2(v[2], v[3])};
    } else if (tt >= 208) {
      const int z = tt - 208;
      const int zo = z < 32 ? LDS_HP + kch * HP_BYTES + ((z >> 4) * 144 + (z & 15)) * 16 : LDS_KC + kch * KC_BYTES + ((z >> 2) & 3) * 4160 + (z & 3) * 16;
      *(u32x4*)(smem + zo) = (u32x4){0, 0, 0, 0};
    }
    lds_post(smem, 20 + kch);
    lds_wait(smem, 20 + kch, 4);
    mid();
    const char* t16 = smem + LDS_T16 + kch * 1408;
    u32 wv[6];
#pragma unroll
    for (int q = 0; q < 6; q++) wv[q] = (u32)*(const u16*)(t16 + (gix[q] & 0xffffu)) | ((u32)*(const u16*)(t16 + (gix[q] >> 16)) << 16);
    u32 al[5];
#pragma unroll
    for (int i = 0; i < 5; i++) al[i] = __builtin_amdgcn_alignbit(wv[i + 1], wv[i], 16);
    char* base = smem + LDS_KC + kch * KC_BYTES + 64 + 16 * tt;
    *(u32x4*)(base) = (u32x4){wv[2], wv[3], wv[4], wv[5]};
    *(u32x4*)(base + 4160) = (u32x4){al[1], al[2], al[3], al[4]};
    *(u32x4*)(base + 2 * 4160) = (u32x4){wv[1], wv[2], wv[3], wv[4]};
    *(u32x4*)(base + 3 * 4160) = (u32x4){al[0], al[1], al[2], al[3]};
  };
  lds_post(smem, 18 + kch);
  if (w < 4) {
    u32x4 gs[4], gx[4];
    const u32 o_g = (OPT_NG ? OFF_NG + (u32)id * (L_ * 8) : OFF_STR + xcd * 16384) + w * 4096 + lane * 16, o_xg = OFF_XG + (u32)id * (L_ * 8) + w * 4096 + lane * 16;
#define LD4X(g, off, tail) asm volatile("global_load_dwordx4 %0, %4, %5 sc1\n\tglobal_load_dwordx4 %1, %4, %5 offset:1024 sc1\n\tglobal_load_dwordx4 %2, %4, %5 offset:2048 sc1\n\tglobal_load_dwordx4 %3, %4, %5 offset:3072 sc1" tail \
      : "+v"(g[0]), "+v"(g[1]), "+v"(g[2]), "+v"(g[3]) : "v"(off), "s"(ws) : "memory")
    auto settle = [&](u32x4 (&g)[4], u32 off) {
      asm volatile("s_waitcnt vmcnt(0)" : "+v"(g[0]), "+v"(g[1]), "+v"(g[2]), "+v"(g[3]) :: "memory");
#pragma nounroll
      for (int sp = 0; sp < SPIN_CAP; sp++) {
        u32 bd = 0;
#pragma unroll
        for (int i = 0; i < 4; i++) bd |= (g[i][1] ^ tag) | (g[i][3] ^ tag);
        if (!__any(bd != 0)) break;
        __builtin_amdgcn_s_sleep(6);
        if (bd != 0) LD4X(g, off, "\n\ts_waitcnt vmcnt(0)");
      }
    };
#pragma unroll
    for (int i = 0; i < 4; i++) { gs[i] = (u32x4){0, 0, 0, 0}; gx[i] = (u32x4){0, 0, 0, 0}; }
    auto sweep = [&]() {
    LD4X(gx, o_xg, ""); LD4X(gs, o_g, "");
    if (OPT_TOUCH) {
      const char* tb = w == 0 ? (const char*)p.w1 : w == 1 ? (const char*)p.w3 : w == 2 ? (const char*)p.w2 : (const char*)p.wm;
      const u32 ot = w < 3 ? (u32)(id * 56 + (lane < 56 ? lane : 0)) * 128 : (u32)(2 * id + (lane & 1)) * (6 * D_ * 2) + (((lane >> 1) % 10) < 3 ? 25 + (lane >> 1) % 10 : 32 + (lane >> 1) % 10) * 128;
      const u32 l_ = lbase + LDS_JUNK;
      asm volatile("s_mov_b32 m0, %2\n\ts_nop 0\n\tglobal_load_lds_dword %0, %1" :: "v"(ot), "s"(tb), "s"(l_) : "memory");
    }
    };
    if (OPT_SWEEP_EARLY == 1) sweep();
    finish_k([&]() { if (OPT_SWEEP_EARLY == 0) sweep(); });
    if (OPT_SWEEP_EARLY == 2) sweep();
    STAMP(16);
    asm volatile("s_waitcnt vmcnt(%8)" : "+v"(gs[0]), "+v"(gs[1]), "+v"(gs[2]), "+v"(gs[3]), "+v"(gx[0]), "+v"(gx[1]), "+v"(gx[2]), "+v"(gx[3]) : "i"(OPT_TOUCH ? 1 : 0) : "memory");
    {
      u32 bd = 0;
#pragma unroll
      for (int i = 0; i < 4; i++) bd |= ((gs[i][1] ^ tag) | (gs[i][3] ^ tag)) | ((gx[i][1] ^ tag) | (gx[i][3] ^ tag));
      if (__builtin_expect(__any(bd != 0), 0)) { settle(gx, o_xg); settle(gs, o_g); }
    }
    if (!OPT_NG) {
#pragma unroll
      for (int i = 0; i < 4; i++)
#pragma unroll
        for (int e = 0; e < 2; e++) {
          const u32 pl = gs[i][2 * e];
          gs[i][2 * e] = pk2((up2(gx[i][2 * e]) - lo_f(pl)) * __builtin_amdgcn_rcpf(hi_f(pl)));
        }
    }
    lds_post(smem, 17);
    lds_wait(smem, 14, 8);
    STAMP(4);
    char* hb = smem + LDS_HP + (((lane >> 2) & 1) * 144 + 16 + 32 * w + (lane >> 3)) * 16 + (lane & 3) * 4;
    char* xb_ = smem + LDS_TX + w * 1024 + lane * 4;
#pragma unroll
    for (int i = 0; i < 4; i++) {
      *(u32*)(hb + i * 128) = __builtin_amdgcn_perm(gs[i][2], gs[i][0], 0x05040100u);
      *(u32*)(hb + i * 128 + HP_BYTES) = __builtin_amdgcn_perm(gs[i][2], gs[i][0], 0x07060302u);
      *(u32*)(xb_ + i * 256) = __builtin_amdgcn_perm(gx[i][2], gx[i][0], 0x05040100u);
      *(u32*)(xb_ + i * 256 + 4096) = __builtin_amdgcn_perm(gx[i][2], gx[i][0], 0x07060302u);
    }
#pragma unroll
    for (int s = 0; s < 3; s++) { DMA_W2(s, 0, LDS_S1 + s * 8192); DMA_W2(s, 8, LDS_S1 + s * 8192 + 4096); }
  } else {
    finish_k([]() {});
    STAMP(16);
    lds_wait(smem, 17, 4);
#pragma unroll
    for (int s = 3; s < 6; s++) { DMA_W2(s, 0, LDS_S1 + s * 8192); DMA_W2(s, -8, LDS_S1 + s * 8192 - 4096); }
    STAMP(4);
  }
  u32 cv_a, cv_b, cv_y;
  {
    const int ch = (w >> 1) & 1, hf = w & 1, r = lane & 15, g = lane >> 4, u0 = 4 * r + g;
    cv_a = lbase + LDS_KC + ch * KC_BYTES + (r >> 2) * 4160 + 2 * (32 + 16 * (1 + 8 * hf) - 8 * g - 4 + 4 * (r & 3));
    cv_b = lbase + LDS_HP + ch * HP_BYTES + ((g & 1) * 144 + 16 + r + (g >> 1) - 7 - 8 * hf) * 16;
    cv_y = lbase + LDS_KC + (hf * 2 + ch) * 8192 + (u0 ^ ((u0 >> 3) & 7)) * 16;
    PIN(cv_a); PIN(cv_b); PIN(cv_y);
  }
  STAMP(17);
  BAR();
  if (b == 0 && t == 0) { u32 z = 0; ST4S(z, tag, ws); }
  STAMP(5);
  u32 wrdA = wrd + LDS_S1, wrdB = wrd + LDS_S1 + 5 * 8192;
  PIN(wrdA); PIN(wrdB);
  auto rd_s1 = [&](int s) { return s < 5 ? trf(wrdA, s * 8192, s * 8192 + 512) : trf(wrdB, (s - 5) * 8192, (s - 5) * 8192 + 512); };
  auto rd_first = [&]() {
#pragma unroll
    for (int s = 0; s < R1; s++) W[s] = rd_s1(s);
#pragma unroll
    for (int s = 0; s < 5; s++) W[R1 + s] = trf(wrd, s * 8192, s * 8192 + 512);
  };
  if (w < 4) {
    f32x4 acc[8] = {};
    u32x4 A[2][8], B[2][8];
    auto ld = [&](int j, int u) {
      A[u][0] = trf(cv_a, 64 * j + 8, 64 * j);
#pragma unroll
      for (int d = 7; d >= 4; d--) B[u][d] = *(const LAS u32x4*)(cv_b + 256 * d + 32 * (3 - j));
#pragma unroll
      for (int a = 1; a < 8; a++) {
        A[u][a] = trf(cv_a, 512 * a + 64 * j + 8, 512 * a + 64 * j);
        if (a <= 4) B[u][4 - a] = *(const LAS u32x4*)(cv_b + 256 * (4 - a) + 32 * (3 - j));
      }
    };
    if (OPT_PRIO) __builtin_amdgcn_s_setprio(2);
    ld(0, 0);
    SCHED();
#pragma unroll
    for (int j = 0; j < 4; j++) {
      const int u = j & 1;
      if (j < 3) ld(j + 1, u ^ 1);
#pragma unroll
      for (int i = 0; i < 8; i++) {
#pragma unroll
        for (int nt = 7; nt >= 4; nt--)
          if (nt >= i) acc[nt] = __builtin_amdgcn_mfma_f32_16x16x32_bf16(asb(A[u][i]), asb(B[u][nt - i]), acc[nt], 0, 0, 0);
#pragma unroll
        for (int nt = 3; nt >= 0; nt--) {
          const int a = i - 7 + nt;
          if (a >= 0) acc[nt] = __builtin_amdgcn_mfma_f32_16x16x32_bf16(asb(A[u][a]), asb(B[u][nt - a]), acc[nt], 0, 0, 0);
        }
      }
#pragma unroll
      for (int i = 0; i < 36; i++) { __builtin_amdgcn_sched_group_barrier(0x8, 1, 0); __builtin_amdgcn_sched_group_barrier(0x100, 1, 0); }
      SCHED();
      if (j == 2) { lds_post(smem, 2); SCHED(); }
    }
    lds_wait(smem, 2, 8);
#pragma unroll
    for (int nt = 0; nt < 8; nt++) *(LAS f32x4*)(cv_y + nt * 1024) = acc[nt];
    if (OPT_PRIO) __builtin_amdgcn_s_setprio(0);
    STAMP(6);
    WAITV(0);
    BAR();
    STAMP(20);
    rd_first();
    lds_post(smem, 3);
    STAMP(21); STAMP(22); STAMP(7);
    lds_wait(smem, 3, 8);
    lds_wait(smem, 1, 5);
#pragma unroll
    for (int k = NRW; k < 14; k++) { DMA_W2(14 + k, 0, LDS_S1 + (k - 5) * 8192); DMA_W2(14 + k, 8, LDS_S1 + (k - 5) * 8192 + 4096); }
    if (w == 1) {
      const u32 of = OFF_W2F + xcd * 128;
#pragma nounroll
      for (int sp = 0; sp < SPIN_CAP; sp++) {
        u32 v; asm volatile("global_load_dword %0, %1, %2 sc1\n\ts_waitcnt vmcnt(0)" : "=&v"(v) : "v"(of), "s"(ws) : "memory");
        if (__all(v == tag)) break;
        __builtin_amdgcn_s_sleep(4);
      }
    }
    WAITV(0);
  } else {
    u32x2 gm2;
    const u32 o_gm2 = OFF_MODG + (u32)(((lane & 3) == 3 ? 4 : (lane & 3)) * 224 + id) * 8;
    u32 dsk2; { const u32 od = id * 4; LD4(dsk2, od, p.dsk); }
    LD8S(gm2, o_gm2, ws);
#pragma unroll
    for (int s = 6; s < R1; s++) { DMA_W2(s, 0, LDS_S1 + s * 8192); DMA_W2(s, -8, LDS_S1 + s * 8192 - 4096); }
    u32x4 xo_raw[2], no_raw[2], kk[2];
#pragma unroll
    for (int c = 0; c < 2; c++) {
      xo_raw[c] = *(const u32x4*)(smem + LDS_TX + c * 4096 + 16 * tt);
      no_raw[c] = *(const u32x4*)(smem + LDS_HP + c * HP_BYTES + ((tt & 1) * 144 + 16 + (tt >> 1)) * 16);
      kk[c] = *(const u32x4*)(smem + LDS_KC + c * KC_BYTES + 64 + 16 * tt);
    }
    lds_post(smem, 2); lds_post(smem, 16);
    lds_wait(smem, 16, 4);
#pragma unroll
    for (int s = 0; s < 5; s++) { DMA_W2(R1 + s, 0, r2slot(s)); DMA_W2(R1 + s, -8, r2slot(s) - 4096); }
    float zs[2][8];
#pragma unroll
    for (int c = 0; c < 2; c++) {
#pragma unroll
      for (int q = 0; q < 4; q++) { zs[c][2 * q] = lo_f(kk[c][q]); zs[c][2 * q + 1] = hi_f(kk[c][q]); }
#pragma unroll
      for (int j = 1; j < 8; j++) zs[c][j] = sadd(zs[c][j], zs[c][j - 1]);
    }
    float ex[2];
#pragma unroll
    for (int c = 0; c < 2; c++) {
      float r = zs[c][7];
      r += dppf<0x111, 0xf>(r); r += dppf<0x112, 0xf>(r); r += dppf<0x114, 0xf>(r); r += dppf<0x118, 0xf>(r); r += dppf<0x142, 0xa>(r); r += dppf<0x143, 0xc>(r);
      ex[c] = r - zs[c][7];
      if (lane == 63) *(float*)(smem + LDS_CNT + 96 + 16 * c + 4 * (w & 3)) = r;
    }
    lds_post(smem, 23);
    asm volatile("s_waitcnt vmcnt(16)" : "+v"(gm2), "+v"(dsk2) :: "memory");
#pragma nounroll
    for (int sp = 0; sp < SPIN_CAP && !__all(gm2[1] == tag); sp++) {
      __builtin_amdgcn_s_sleep(8);
      asm volatile("global_load_dwordx2 %0, %1, %2 sc1\n\ts_waitcnt vmcnt(0)" : "=&v"(gm2) : "v"(o_gm2), "s"(ws) : "memory");
    }
    u32x4 RW[2 * NRW];
#pragma unroll
    for (int i = 0; i < 2 * NRW; i++) {
      const char* a_ = wbase + (int64_t)(16 * KS(14 + i / 2) - 8 * (i & 1)) * (F_ * 2);
      LD16(RW[i], wlane, a_);
    }
    const u32 msa = __builtin_amdgcn_readlane(gm2[0], 0), mca = __builtin_amdgcn_readlane(gm2[0], 1);
    const u32 mga = __builtin_amdgcn_readlane(gm2[0], 2), mcb = __builtin_amdgcn_readlane(gm2[0], 3);
    const f32x2 sa = up2(msa), c1 = up2(pk2(up2(mca) + 1.f)), ga = up2(mga), c1b = up2(pk2(up2(mcb) + 1.f)), dk = up2(dsk2);
    lds_wait(smem, 23, 4);
    float hd[2][8];
#pragma unroll
    for (int c = 0; c < 2; c++) {
      const f32x4 wt = *(const f32x4*)(smem + LDS_CNT + 96 + c * 16);
      const int wq = w & 3;
      const float off = (ex[c] + (wq > 0 ? wt[0] : 0.f) + (wq > 1 ? wt[1] : 0.f) + (wq > 2 ? wt[2] : 0.f)) * sa[c];
      const float ka = c1[c] * dk[c], kb = sa[c] * dk[c];
#pragma unroll
      for (int j = 0; j < 8; j++) {
        zs[c][j] = sfma(zs[c][j], sa[c], off);
        hd[c][j] = sfma((j & 1) ? hi_f(no_raw[c][j >> 1]) : lo_f(no_raw[c][j >> 1]), ka, kb);
        PIN(zs[c][j]); PIN(hd[c][j]);
      }
    }
    STAMP(6);
    WAITV(2 * NRW);
    BAR();
    STAMP(20);
    const int ua = (2 * tt) ^ ((tt >> 2) & 7);
#pragma unroll
    for (int c = 0; c < 2; c++) {
      const char* yr = smem + LDS_KC + c * 8192;
      const f32x4 ya = *(const f32x4*)(yr + ua * 16) + *(const f32x4*)(yr + 16384 + ua * 16);
      const f32x4 yb = *(const f32x4*)(yr + (ua ^ 1) * 16) + *(const f32x4*)(yr + 16384 + (ua ^ 1) * 16);
      const f32x2 yv[4] = {{ya[0], ya[1]}, {ya[2], ya[3]}, {yb[0], yb[1]}, {yb[2], yb[3]}};
      u32x4 ox, os;
#pragma unroll
      for (int q = 0; q < 4; q++) {
        const f32x2 y = up2(pk2(yv[q] * c1[c] + (f32x2){zs[c][2 * q], zs[c][2 * q + 1]}));
        const f32x2 a = up2(pk2(y + (f32x2){hd[c][2 * q], hd[c][2 * q + 1]}));
        const f32x2 m = up2(pk2(a * ga[c]));
        ox[q] = pk2(up2(xo_raw[c][q]) + m);
        os[q] = pk2(up2(ox[q]) * c1b[c]);
      }
      const u32 o_x1 = (u32)(2 * id + c) * (L_ * 2) + tt * 16;
      ST16S(o_x1, os, ws + OFF_X1S); ST16S(o_x1, ox, ws + OFF_X1T);
    }
    rd_first();
    lds_post(smem, 3);
    STAMP(21);
    WAITV(0);
    STAMP(22);
    {
      u32 old; const u32 c = lbase + LDS_CNT + 4 * 1;
      asm volatile("s_mov_b64 exec, 1\n\tds_add_rtn_u32 %0, %1, %2\n\ts_mov_b64 exec, -1\n\ts_waitcnt lgkmcnt(0)" : "=&v"(old) : "v"(c), "v"(1u) : "memory");
      if (rfl(old) == 3u) { if (lane < 8) { u32 o = OFF_CTR + (u32)(lane * 16 + (id & 15)) * 128; ATOM_INC(o, ws); } lds_post(smem, 1); }
    }
    STAMP(7);
    lds_wait(smem, 3, 8);
    u32 d3 = lbase + LDS_R3 + w * 1024 + lane * 16 - 4096, d1 = lbase + LDS_S1 + w * 1024 + lane * 16 - 4096, d1b = d1 + 5 * 8192;
    PIN(d3); PIN(d1); PIN(d1b);
#pragma unroll
    for (int i = 0; i < 2 * NRW; i++) {
      const int k = i / 2, o = (i & 1) ? 0 : 4096;
      PIN(RW[i]);
      *(LAS u32x4*)(k < 5 ? d3 + k * 8192 + o : (k < 10 ? d1 + (k - 5) * 8192 + o : d1b + (k - 10) * 8192 + o)) = RW[i];
    }
  }
  const int p2 = 2 * xcd + ((bi >> 1) & 1), e2 = bi & 1, dg = bi >> 2;
  const u32 o_w2 = OFF_W2T + (u32)((2 * dg * 112 + 14 * w) * 64 + lane) * 16;
  const int rot = OPT_ROT2 == 1 ? 2 * dg : (OPT_ROT2 == 2 ? (xcd * 7) / 4 : 0);
#define KROT(s) (((s) + rot >= 14 ? (s) + rot - 14 : (s) + rot) * 1024)
  u32x4 W2[28];
#define LD_W2(i) do { const char* bs_ = ws + ((i) & 1) * 112 * 1024 + KROT((i) >> 1); LD16S(W2[i], o_w2, bs_); } while (0)
  u32 w3 = wrd + LDS_R3; PIN(w3);
  u32 o_xp = OFF_X1S + (u32)(lane >> 3) * (L_ * 2) + (128 * pm) * 2 + ((lane & 7) ^ ((lane >> 2) & 4)) * 16;
  const int xrow0 = 8 * w + 16 * krot;
#define DMA_X(i) do { int r_ = xrow0 + ((i) % 7) * 64; r_ = r_ >= 448 ? r_ - 448 : r_; const char* bs_ = ws + (size_t)r_ * (L_ * 2) + ((i) / 7) * 128; \
    u32 l_ = lbase + (i) * 8192 + w * 1024; DMAS(o_xp, bs_, l_); } while (0)
  u32 xrdA = lbase + (lane >> 5) * 1024 + ((lane & 15) >> 2) * 128 + ((lane >> 4) & 1) * 32 + (lane & 3) * 8 + ((lane >> 3) & 1) * 64;
  u32 xrdB = xrdA ^ 64;
  u32 xrdC = xrdA + 57344, xrdD = xrdB + 57344;
  u32x2 st2[4], csg;
  u32x4 Wx;
  u32 o_s2 = OFF_ST2G + (u32)(128 * pm + (lane & 31)) * 8;
  u32 o_h = OFF_H + (u32)((pm * 4 * 112 + cg * 8 + w) * 64 + lane) * 16;
  u32 o_cs = OFF_CSG + (u32)((lane >> 5) * 3584 + ((lane >> 4) & 1) * F_ + 128 * cg + 16 * w + ((lane & 3) | ((lane & 8) >> 1) | ((lane & 4) << 1))) * 8;
  PIN(o_xp); PIN(xrdA); PIN(xrdB); PIN(xrdC); PIN(xrdD); PIN(o_s2); PIN(o_h); PIN(o_cs);
  if (w == 7) poll_eq14(ws, OFF_CTR + (u32)(xcd * 16 + (lane & 15)) * 128);
  BAR();
  STAMP(8);
#pragma unroll
  for (int i = 0; i < 4; i++) DMA_X(i);
#pragma unroll
  for (int s = 0; s < 5; s++) W[14 + s] = trf(w3, s * 8192, s * 8192 + 512);
#pragma unroll
  for (int s = 0; s < 9; s++) W[19 + s] = rd_s1(s);
#define LD_ST2(tail) asm volatile("global_load_dwordx2 %0, %5, %7 sc1\n\tglobal_load_dwordx2 %1, %5, %7 offset:256 sc1\n\tglobal_load_dwordx2 %2, %5, %7 offset:512 sc1\n\tglobal_load_dwordx2 %3, %5, %7 offset:768 sc1\n\tglobal_load_dwordx2 %4, %6, %7 sc1" tail \
               : "=&v"(st2[0]), "=&v"(st2[1]), "=&v"(st2[2]), "=&v"(st2[3]), "=&v"(csg) : "v"(o_s2), "v"(o_cs), "s"(ws) : "memory")
  f32x16 a0 = {}, a1 = {}, a2 = {}, a3 = {};
  u32x4 XA[3], XB[3], X[4];
  {
    WAITV(3); BAR();
#pragma unroll
    for (int s = 0; s < 2; s++) { XA[s] = trf(xrdA, s * 2048, s * 2048 + 512); XB[s] = trf(xrdB, s * 2048, s * 2048 + 512); }
    static_for<0, 28>([&](auto S) {
      constexpr int s = decltype(S)::value;
      if constexpr (s + 2 < 28) {
        if constexpr (((s + 2) & 3) == 0) { WAITV(pp0_wait((s + 2) / 4)); BAR_RAW(); }
        XA[(s + 2) % 3] = trf(xrdA, (s + 2) * 2048, (s + 2) * 2048 + 512);
        XB[(s + 2) % 3] = trf(xrdB, (s + 2) * 2048, (s + 2) * 2048 + 512);
      } else {
        if constexpr (s == 26) { asm volatile("s_waitcnt vmcnt(3)" : "+v"(csg) :: "memory"); BAR_RAW(); }
        X[s - 26] = trf(xrdC, (s - 26) * 2048, (s - 26) * 2048 + 512);
        if constexpr (s == 27) X[2] = trf(xrdC, 4096, 4096 + 512);
        if constexpr (s == 26) {
#pragma unroll
          for (int m = 0; m < 4; m++) st2[m] = *(const u32x2*)(smem + LDS_ST2 + m * 256 + (lane & 31) * 8);
        }
      }
      a0 = __builtin_amdgcn_mfma_f32_32x32x16_bf16(asb(W[s]), asb(XA[s % 3]), a0, 0, 0, 0);
      a1 = __builtin_amdgcn_mfma_f32_32x32x16_bf16(asb(W[s]), asb(XB[s % 3]), a1, 0, 0, 0);
      PIN(a0); PIN(a1);
      if constexpr ((s & 1) == 0 && s <= 18) DMA_X(4 + s / 2);
      if constexpr ((s & 3) == 3 && s >= 7 && s <= 23) {
        constexpr int j = (s - 7) / 4;
        const char* bs_ = ws + ((23 + j) & 1) * 112 * 1024 + KROT((23 + j) >> 1); u32 l_ = lbase + LDS_W2 + (w * 5 + j) * 1024; DMAS(o_w2, bs_, l_);
      }
      if constexpr ((s & 3) == 1 && s >= 5 && s <= 25) LD_W2((s - 5) / 4);
      if constexpr (s == 20) {
        const u32 og = OFF_ST2G + (u32)(128 * pm + 16 * w) * 8 + lane * 16, l_ = lbase + LDS_ST2 + w * 128;
        asm volatile("s_mov_b32 m0, %2\n\ts_mov_b64 exec, 0xff\n\tglobal_load_lds_dwordx4 %0, %1 sc1\n\ts_mov_b64 exec, -1" :: "v"(og), "s"(ws), "s"(l_) : "memory");
        LD8S(csg, o_cs, ws);
      }
      SCHED();
    });
  }
  STAMP(23);
  auto fin = [&](f32x16& acc, int mt) {
    const u32 pl = st2[mt][0];
    const u32 e0 = lane < 32 ? ((pl ^ 0x8000u) & 0xffffu) : (pl >> 16);
    const u32x4 ex = {e0 | (e0 << 16), e0, 0u, 0u};
    acc = __builtin_amdgcn_mfma_f32_32x32x16_bf16(asb(Wx), asb(ex), acc, 0, 0, 0);
  };
  float ec1[4], esq[4];
  f32x2 ee, epr;
  u32x4 ho;
  auto stage = [&](const f32x16& acc, int mt, int j, int st) {
    if (st == 0) {
      const float a0_ = smul(acc[2 * j], ec1[mt]), a1_ = smul(acc[2 * j + 1], ec1[mt]);
      epr = (f32x2){smul(acc[2 * j], acc[8 + 2 * j]), smul(acc[2 * j + 1], acc[9 + 2 * j])};
      ee = (f32x2){__builtin_amdgcn_exp2f(a0_), __builtin_amdgcn_exp2f(a1_)};
      PIN(ee); PIN(epr);
    } else if (st == 1) {
      const float d0 = sfma(ee[0], esq[mt], esq[mt]), d1 = sfma(ee[1], esq[mt], esq[mt]);
      ee = (f32x2){__builtin_amdgcn_rcpf(d0), __builtin_amdgcn_rcpf(d1)};
      PIN(ee);
    } else {
      u32 v = pack2(smul(epr[0], ee[0]), smul(epr[1], ee[1])); PIN(v);
      ho[j] = v;
    }
  };
  auto put = [&](u32x4 o, int mt) { const char* bs = ws + (size_t)mt * (112 * 1024); ST16S(o_h, o, bs); };
  auto signal3 = [&](int half) {
    u32 old; const u32 c = lbase + LDS_CNT + 4 * (half == 2 ? 22 : half);
    asm volatile("s_mov_b64 exec, 1\n\tds_add_rtn_u32 %0, %1, %2\n\ts_mov_b64 exec, -1\n\ts_waitcnt lgkmcnt(0)" : "=&v"(old) : "v"(c), "v"(1u) : "memory");
    if (rfl(old) == 7u) {
      const u32 o = OFF_CTR + (u32)(128 + 3 * pm + half) * 128;
      asm volatile("s_mov_b64 exec, 1\n\tglobal_atomic_add %0, %1, %2\n\ts_mov_b64 exec, -1" :: "v"(o), "v"(1u), "s"(ws) : "memory");
    }
  };
  auto post3 = [&](int half) { lds_arrive(smem, 12 + half, lane); };
  auto wait3 = [&](bool ok, int half) {
    if (w == PW) {
      if (!ok) poll_eq14(ws, OFF_CTR + (u32)(128 + 3 * p2 + (half ? 1 + e2 : 0)) * 128);
      post3(half);
    } else if (!ok) lds_wait(smem, 12 + half, 1);
  };
#define LD_S3(half) do { if (w == PW) { const char* fb_ = ws + OFF_CTR + (128 + 3 * p2 + ((half) ? 1 + e2 : 0)) * 128; const u32 l_ = lbase + LDS_JUNK; \
    asm volatile("s_mov_b32 m0, %2\n\ts_nop 0\n\tglobal_load_lds_dword %0, %1 sc1" :: "v"(zero_off), "s"(fb_), "s"(l_) : "memory"); } } while (0)
#define S3_OK() (lds_peek(lbase + LDS_JUNK) == 14u)
  const u32 zero_off = 0;
  u32x4 Hf[8];
  bool pre = false; u32 s3w = 0;
  auto sig = [&](int half) {
    if (w == PW) {
      const u32 o = OFF_CTR + (u32)(128 + 3 * pm + half) * 128;
      asm volatile("s_mov_b64 exec, 1\n\tglobal_atomic_add %0, %1, %2\n\ts_mov_b64 exec, -1" :: "v"(o), "v"(1u), "s"(ws) : "memory");
    }
  };
  const u32 o_hl = OFF_H + (u32)(((4 * p2 + e2) * 112 + 14 * w) * 64 + lane) * 16;
  auto h_pre = [&](int s) {
    const char* bs = ws + KROT(s); u32 l_ = lbase + w * 7168 + s * 1024; DMAS(o_hl, bs, l_);
  };
  {
    auto item = [&](int k) {
      const int n = k / 3, st = k % 3;
      if (n < 4) stage(a0, 0, n, st); else if (n < 8) stage(a1, 1, n - 4, st); else stage(a2, 2, n - 8, st);
      if (st == 2 && (n & 3) == 3) put(ho, n >> 2);
    };
    static_for<0, 56>([&](auto S) {
      constexpr int u = decltype(S)::value, s = u % 28;
      if constexpr (u + 3 < 28) X[(u + 3) & 3] = trf(xrdC, (u + 3) * 2048, (u + 3) * 2048 + 512);
      else if constexpr (u + 3 < 56) X[(u + 3) & 3] = trf(xrdD, (u - 25) * 2048, (u - 25) * 2048 + 512);
      if constexpr (u < 28) { a2 = __builtin_amdgcn_mfma_f32_32x32x16_bf16(asb(W[s]), asb(X[u & 3]), a2, 0, 0, 0); PIN(a2); }
      else { a3 = __builtin_amdgcn_mfma_f32_32x32x16_bf16(asb(W[s]), asb(X[u & 3]), a3, 0, 0, 0); PIN(a3); }
      if constexpr (u == 0) {
        if (!__all(st2[0][1] == tag && st2[1][1] == tag && st2[2][1] == tag && st2[3][1] == tag && csg[1] == tag)) {
          WAITV(0);
#pragma nounroll
          for (int sp = 0; sp < SPIN_CAP; sp++) {
            __builtin_amdgcn_s_sleep(4);
            LD_ST2("\n\ts_waitcnt vmcnt(0)");
            if (__all(st2[0][1] == tag && st2[1][1] == tag && st2[2][1] == tag && st2[3][1] == tag && csg[1] == tag)) break;
          }
        }
        const float tot = __uint_as_float(csg[0]);
        const float h0 = rb(tot), h1 = rb(tot - h0), h2 = tot - h0 - h1;
        Wx = (u32x4){pack2(h0, h1), pack2(h2, 0.f), 0u, 0u};
        fin(a0, 0);
      }
      if constexpr (u == 1) {
        fin(a1, 1);
#pragma unroll
        for (int m = 0; m < 4; m++) { const float sd = hi_f(st2[m][0]); ec1[m] = -1.44269504088896341f * __builtin_amdgcn_rcpf(sd); esq[m] = sd * sd; }
      }
      if constexpr (u == 28) fin(a2, 2);
      if constexpr (u >= 3 && u < 21) {
        constexpr int k0 = 4 * ((u - 3) / 3) + (u - 3) % 3;
        item(k0);
        if constexpr ((u - 3) % 3 == 2) { SCHED(); item(k0 + 1); }
      }
      if constexpr (u >= 31 && u < 43) item(24 + u - 31);
      if constexpr (u >= 2 && u < 28 && (u % 3) == 1) LD_W2(5 + u / 3);
      if constexpr (u >= 28 && u < 46 && (u & 1) == 0) LD_W2(14 + (u - 28) / 2);
      if constexpr (u == 35) { WAITV(6); BAR_RAW(); sig(0); }
      if constexpr (u == 46) LD_S3(0);
      SCHED();
    });
    STAMP(28);
    fin(a3, 3);
    WAITV(0); BAR_RAW(); sig(1);
    pre = lds_peek(lbase + LDS_JUNK) == 14u;
    if (pre) {
#pragma unroll
      for (int s = 0; s < 4; s++) h_pre(s);
    }
    f32x2 e4[4], p4[4];
#pragma unroll
    for (int j = 0; j < 4; j++) {
      const f32x2 gg = {a3[2 * j], a3[2 * j + 1]}, uu = {a3[8 + 2 * j], a3[9 + 2 * j]};
      const f32x2 ar = gg * ec1[3];
      p4[j] = gg * uu;
      e4[j] = (f32x2){__builtin_amdgcn_exp2f(ar[0]), __builtin_amdgcn_exp2f(ar[1])};
    }
#pragma unroll
    for (int j = 0; j < 4; j++) {
      const f32x2 den = __builtin_elementwise_fma(e4[j], (f32x2){esq[3], esq[3]}, (f32x2){esq[3], esq[3]});
      ho[j] = pk2(p4[j] * (f32x2){__builtin_amdgcn_rcpf(den[0]), __builtin_amdgcn_rcpf(den[1])});
    }
    put(ho, 3);
  }
  STAMP(29);
  WAITV(0);
  signal3(2);
#pragma unroll
  for (int i = 0; i < 23; i++) PIN(W2[i]);
  if (!pre) {
    wait3(false, 0);
#pragma unroll
    for (int s = 0; s < 4; s++) h_pre(s);
  }
#pragma unroll
  for (int s = 4; s < 7; s++) h_pre(s);
  STAMP(11);
#define LD_H(u) do { const char* bs_ = ws + ((u) / 14) * 2 * 112 * 1024 + KROT((u) % 14); LD16S(Hf[(u) & 7], o_hl, bs_); } while (0)
#pragma unroll
  for (int u = 7; u < 14; u++) LD_H(u);
  LD_S3(1);
#pragma unroll
  for (int j = 0; j < 5; j++) W2[23 + j] = *(const u32x4*)(smem + LDS_W2 + (w * 5 + j) * 1024 + lane * 16);
  const int ocol = 64 * dg + 32 * (w >> 2) + 8 * (w & 3) + 4 * (lane >> 5);
  const int orow = 128 * p2 + 32 * e2 + (lane & 31);
  u32 xres[8]; u32x4 gbg;
  const u32 o_gb = OFF_MODG + (u32)(5 * 224 + (ocol >> 1)) * 8;
  u32 rw0 = lbase + w * 1024 + lane * 16, rw1 = rw0 + 65536, rr0 = lbase + w * 8192 + lane * 16, rr1 = rr0 + 65536;
  PIN(rw0); PIN(rw1); PIN(rr0); PIN(rr1);
  auto dump = [&](u32 base, const f32x16& f, int o) {
    const int i0 = 4 * (o & 3);
    *(LAS f32x4*)(base + o * 8192) = (f32x4){f[i0], f[i0 + 1], f[i0 + 2], f[i0 + 3]};
  };
  auto gather = [&](u32 base) {
    f32x4 v[8];
#pragma unroll
    for (int i = 0; i < 8; i++) v[i] = *(const LAS f32x4*)(base + i * 1024);
    return ((v[0] + v[1]) + (v[2] + v[3])) + ((v[4] + v[5]) + (v[6] + v[7]));
  };
  f32x16 f0 = {}, f1 = {}, g0 = {}, g1 = {};
  WAITV(7);
  {
    u32x4 Hl[7];
#pragma unroll
    for (int s = 0; s < 7; s++) Hl[s] = *(const u32x4*)(smem + w * 7168 + s * 1024 + lane * 16);
#pragma unroll
    for (int s = 0; s < 7; s++) {
      f0 = __builtin_amdgcn_mfma_f32_32x32x16_bf16(asb(W2[2 * s]), asb(Hl[s]), f0, 0, 0, 0);
      f1 = __builtin_amdgcn_mfma_f32_32x32x16_bf16(asb(W2[2 * s + 1]), asb(Hl[s]), f1, 0, 0, 0);
    }
    PIN(f0); PIN(f1);
  }
  lds_arrive(smem, 15, lane);
  {
    bool ok1 = false;
    if (w == PW) { WAITV(0); ok1 = S3_OK(); }
    wait3(ok1, 1);
  }
  STAMP(12);
  LD_H(14);
  static_for<7, 28>([&](auto U) {
    constexpr int u = decltype(U)::value, s = u % 14;
    constexpr int last = u + 7 > 27 ? 27 : u + 7;
    asm volatile("s_waitcnt vmcnt(%1)" : "+v"(Hf[u & 7]) : "i"(last - u + (u >= 20 ? 9 : 0)) : "memory");
    if constexpr (u < 14) {
      f0 = __builtin_amdgcn_mfma_f32_32x32x16_bf16(asb(W2[2 * s]), asb(Hf[u & 7]), f0, 0, 0, 0);
      f1 = __builtin_amdgcn_mfma_f32_32x32x16_bf16(asb(W2[2 * s + 1]), asb(Hf[u & 7]), f1, 0, 0, 0);
      PIN(f0); PIN(f1);
    } else {
      g0 = __builtin_amdgcn_mfma_f32_32x32x16_bf16(asb(W2[2 * s]), asb(Hf[u & 7]), g0, 0, 0, 0);
      if constexpr (u < 25) g1 = __builtin_amdgcn_mfma_f32_32x32x16_bf16(asb(W2[2 * s + 1]), asb(Hf[u & 7]), g1, 0, 0, 0);
      PIN(g0); PIN(g1);
    }
    if constexpr (u + 8 <= 27) LD_H(u + 8);
    if constexpr (u == 19) {
      const u32 o = OFF_X1T + (u32)ocol * (L_ * 2) + orow * 2;
#pragma unroll
      for (int j = 0; j < 8; j++) { const char* bs = ws + (j & 3) * (L_ * 2) + (j >> 2) * 128; LD2S(xres[j], o, bs); }
      LD16S(gbg, o_gb, ws);
    }
    if constexpr (u == 14) { STAMP(30); lds_wait(smem, 15, 8); }
    if constexpr (u >= 15 && u < 19) { dump(rw0, f0, u - 15); dump(rw0, f1, 4 + u - 15); }
    SCHED();
  });
  static_for<25, 28>([&](auto U) {
    constexpr int u = decltype(U)::value, s = u % 14;
    g1 = __builtin_amdgcn_mfma_f32_32x32x16_bf16(asb(W2[2 * s + 1]), asb(Hf[u & 7]), g1, 0, 0, 0);
    PIN(g1);
    if constexpr (u == 26) { dump(rw1, g0, 0); dump(rw1, g0, 1); }
    if constexpr (u == 27) { dump(rw1, g0, 2); dump(rw1, g0, 3); }
    SCHED();
  });
  STAMP(13);
#pragma unroll
  for (int o = 4; o < 8; o++) dump(rw1, g1, o);
  asm volatile("s_waitcnt vmcnt(0)" : "+v"(xres[0]), "+v"(xres[1]), "+v"(xres[2]), "+v"(xres[3]), "+v"(xres[4]), "+v"(xres[5]), "+v"(xres[6]), "+v"(xres[7]), "+v"(gbg) :: "memory");
#pragma nounroll
  for (int sp = 0; sp < SPIN_CAP; sp++) {
    if (__all(gbg[1] == tag && gbg[3] == tag)) break;
    __builtin_amdgcn_s_sleep(4);
    asm volatile("global_load_dwordx4 %0, %1, %2 sc1\n\ts_waitcnt vmcnt(0)" : "=&v"(gbg) : "v"(o_gb), "s"(ws) : "memory");
  }
  BAR();
#pragma unroll
  for (int hf = 0; hf < 2; hf++) {
    const f32x4 sum = gather(hf ? rr1 : rr0);
    u32x2 res;
#pragma unroll
    for (int j = 0; j < 2; j++) {
      const f32x2 f = up2(pack2(sum[2 * j], sum[2 * j + 1]));
      const f32x2 m = up2(pk2(f * up2(gbg[2 * j])));
      res[j] = pk2((f32x2){lo_f(xres[4 * hf + 2 * j]), lo_f(xres[4 * hf + 2 * j + 1])} + m);
    }
    *(u32x2*)(p.out + (size_t)(orow + 64 * hf) * D_ + ocol) = res;
  }
  STAMP(14);
  STAMP_OUT;
}

extern "C" void kernel_launch(void* const* d_in, const int* in_sizes, int n_in,
                              void* d_out, int out_size, void* d_ws, size_t ws_size,
                              hipStream_t stream) {
  Params p;
  p.x = (const u16*)d_in[0]; p.lar = (const u16*)d_in[1]; p.aim = (const u16*)d_in[2];
  p.cr = (const u16*)d_in[3]; p.ci = (const u16*)d_in[4]; p.ldt = (const u16*)d_in[5]; p.dsk = (const u16*)d_in[6];
  p.w1 = (const u16*)d_in[8]; p.w3 = (const u16*)d_in[9]; p.w2 = (const u16*)d_in[10];
  p.c = (const u16*)d_in[11]; p.wm = (const u16*)d_in[12]; p.ws = (char*)d_ws; p.out = (u16*)d_out;
  fused_kernel<<<256, 512, 0, stream>>>(p);
}
```

```cpp
#include <hip/hip_runtime.h>
#include <stdint.h>
#include <type_traits>

typedef unsigned short u16;
typedef unsigned int u32;
typedef __attribute__((ext_vector_type(8))) __bf16 bf16x8;
typedef __attribute__((ext_vector_type(2))) __bf16 bf16x2;
typedef __attribute__((ext_vector_type(8))) _Float16 f16x8;
typedef __attribute__((ext_vector_type(2))) _Float16 f16x2;
typedef __attribute__((ext_vector_type(4))) short s16x4;
typedef __attribute__((ext_vector_type(2))) float f32x2;
typedef __attribute__((ext_vector_type(4))) float f32x4;
typedef __attribute__((ext_vector_type(16))) float f32x16;
typedef __attribute__((ext_vector_type(4))) u32 u32x4;
typedef __attribute__((ext_vector_type(2))) u32 u32x2;
#define LAS __attribute__((address_space(3)))
#define DEV __device__ __forceinline__

constexpr int L_ = 2048, D_ = 448, N_ = 256, F_ = 1792;
constexpr u32 OFF_E    = 0;
constexpr u32 OFF_CTR  = 4096;
constexpr u32 OFF_W2F  = OFF_CTR + 177 * 128;
constexpr u32 OFF_STG  = 32768;
constexpr u32 OFF_MODG = 49152;
constexpr u32 OFF_ST2G = 65536;
constexpr u32 OFF_CSG  = 81920;
constexpr u32 OFF_XG   = 147456;
constexpr u32 OFF_NG   = OFF_XG + 224 * L_ * 8;
constexpr u32 OFF_X1T  = OFF_NG + 224 * L_ * 8;
constexpr u32 OFF_X1S  = OFF_X1T + D_ * L_ * 2;
constexpr u32 OFF_W2T  = OFF_X1S + D_ * L_ * 2;
constexpr u32 OFF_H    = OFF_W2T + 14 * 112 * 1024;
constexpr u32 OFF_STR  = OFF_H + 64 * 112 * 1024;
constexpr size_t OFF_DBG = (size_t)64 << 20;
constexpr int SPIN_CAP = 1 << 14;

#define OPT_CODEPF 1
#define OPT_TLB 0
#define OPT_ROT 2
#define OPT_ROT2 2
#define OPT_PREW 0
#define OPT_TOUCH 0
#define OPT_PRIO 1
#define OPT_SWEEP_EARLY 0
#define OPT_NOW2 0
#define OPT_PBAR 14
#define OPT_GATE 17
#define PW 7
#define OPT_NG 0
#define NRW 7
#define OPT_LNPOS 1
#define N0 3
#define BW 7
#define CHK 12
#define DENS 0
#define NOBAR 0
#define PRS 2
#define OUTM 0
constexpr int R1 = 9, R2 = 14, R3 = 5;
constexpr int KGW = 4352;
constexpr int LDS_KG  = 0;
constexpr int LDS_T16 = 8 * KGW;
constexpr int LDS_TX  = LDS_T16 + 2816;
constexpr int KC_BYTES = 4 * 4160, HP_BYTES = 2 * 144 * 16;
constexpr int LDS_KC  = LDS_TX + 8192;
constexpr int LDS_HP  = LDS_KC + 2 * KC_BYTES;
constexpr int LDS_S1  = LDS_HP + 2 * HP_BYTES;
constexpr int LDS_CNT = 163840 - 128;
constexpr int LDS_TOTAL = 163840;
constexpr int LDS_R3  = 32768;
constexpr int LDS_JUNK = LDS_CNT - 256;
constexpr int r2slot(int s) { return s < 5 ? s * 8192 : LDS_S1 + (s - 5) * 8192; }
static_assert(5 * 8192 <= LDS_KC && LDS_S1 + 9 * 8192 <= LDS_CNT && LDS_R3 + R3 * 8192 <= LDS_S1 && R1 + R2 + R3 == 28 && (LDS_S1 & 15) == 0, "lds map");
constexpr int CODE_BYTES = 44000;

#define STAMP(i) SCHED()
#define STAMP_DECL do {} while (0)
#define STAMP_OUT do {} while (0)

DEV u32 pack2(float lo, float hi) { f32x2 v = {lo, hi}; bf16x2 b = __builtin_convertvector(v, bf16x2); return __builtin_bit_cast(u32, b); }
DEV u32 packh(float lo, float hi) { f32x2 v = {lo, hi}; f16x2 b = __builtin_convertvector(v, f16x2); return __builtin_bit_cast(u32, b); }
DEV float lo_f(u32 u) { return __uint_as_float(u << 16); }
DEV float hi_f(u32 u) { return __uint_as_float(u & 0xffff0000u); }
DEV float rb(float f) { return lo_f(pack2(f, f)); }
DEV float dot2(u32 a, u32 b, float c) { return __builtin_amdgcn_fdot2_f32_bf16(__builtin_bit_cast(bf16x2, a), __builtin_bit_cast(bf16x2, b), c, false); }
DEV bf16x8 asb(u32x4 v) { return __builtin_bit_cast(bf16x8, v); }
DEV f16x8 ash(u32x4 v) { return __builtin_bit_cast(f16x8, v); }
DEV float fexp(float x) { return __builtin_amdgcn_exp2f(x * 1.44269504088896341f); }
DEV u32 rfl(u32 v) { return __builtin_amdgcn_readfirstlane(v); }

template <int CTRL, int RMASK> DEV float dppf(float v) { return __int_as_float(__builtin_amdgcn_update_dpp(0, __float_as_int(v), CTRL, RMASK, 0xf, false)); }
DEV float wave_sum(float v) {
  v += dppf<0x111, 0xf>(v); v += dppf<0x112, 0xf>(v); v += dppf<0x114, 0xf>(v); v += dppf<0x118, 0xf>(v);
  v += dppf<0x142, 0xa>(v); v += dppf<0x143, 0xc>(v);
  return __int_as_float(__builtin_amdgcn_readlane(__float_as_int(v), 63));
}
template <int CTRL, int RMASK> DEV u32 dppmax(u32 v) { u32 o = (u32)__builtin_amdgcn_update_dpp(0, (int)v, CTRL, RMASK, 0xf, false); return o > v ? o : v; }
DEV u32 wave_maxu(u32 v) {
  v = dppmax<0x111, 0xf>(v); v = dppmax<0x112, 0xf>(v); v = dppmax<0x114, 0xf>(v); v = dppmax<0x118, 0xf>(v);
  v = dppmax<0x142, 0xa>(v); v = dppmax<0x143, 0xc>(v);
  return __builtin_amdgcn_readlane(v, 63);
}
DEV float red32(float a, float b) { auto r = __builtin_amdgcn_permlane32_swap(__float_as_uint(a), __float_as_uint(b), false, false); return __uint_as_float(r[0]) + __uint_as_float(r[1]); }
DEV float red16(float a, float b) { auto r = __builtin_amdgcn_permlane16_swap(__float_as_uint(a), __float_as_uint(b), false, false); return __uint_as_float(r[0]) + __uint_as_float(r[1]); }

#define LD16(d, off, base)    asm volatile("global_load_dwordx4 %0, %1, %2" : "=v"(d) : "v"(off), "s"(base) : "memory")
#define LD16S(d, off, base)   asm volatile("global_load_dwordx4 %0, %1, %2 sc1" : "=v"(d) : "v"(off), "s"(base) : "memory")
#define LD8S(d, off, base)    asm volatile("global_load_dwordx2 %0, %1, %2 sc1" : "=v"(d) : "v"(off), "s"(base) : "memory")
#define LD4(d, off, base)     asm volatile("global_load_dword %0, %1, %2" : "=v"(d) : "v"(off), "s"(base) : "memory")
#define LD4S(d, off, base)    asm volatile("global_load_dword %0, %1, %2 sc1" : "=v"(d) : "v"(off), "s"(base) : "memory")
#define LD2(d, off, base)     asm volatile("global_load_ushort %0, %1, %2" : "=v"(d) : "v"(off), "s"(base) : "memory")
#define LD2S(d, off, base)    asm volatile("global_load_ushort %0, %1, %2 sc1" : "=v"(d) : "v"(off), "s"(base) : "memory")
#define ST16S(off, v, base)   asm volatile("global_store_dwordx4 %0, %1, %2 sc1\n\ts_nop 1" :: "v"(off), "v"(v), "s"(base) : "memory")
#define ST8S(off, v, base)    asm volatile("global_store_dwordx2 %0, %1, %2 sc1" :: "v"(off), "v"(v), "s"(base) : "memory")
#define ST4S(off, v, base)    asm volatile("global_store_dword %0, %1, %2 sc1" :: "v"(off), "v"(v), "s"(base) : "memory")
#define ATOM_INC(off, base)   asm volatile("global_atomic_add %0, %1, %2" :: "v"(off), "v"(1u), "s"(base) : "memory")
#define DMA(off, base, lds)   asm volatile("s_mov_b32 m0, %2\n\ts_nop 0\n\tglobal_load_lds_dwordx4 %0, %1" :: "v"(off), "s"(base), "s"(lds) : "memory")
#define DMAS(off, base, lds)  asm volatile("s_mov_b32 m0, %2\n\ts_nop 0\n\tglobal_load_lds_dwordx4 %0, %1 sc1" :: "v"(off), "s"(base), "s"(lds) : "memory")
#define WAITV(n)              asm volatile("s_waitcnt vmcnt(%0)" :: "i"(n) : "memory")
#define PIN(x)                asm volatile("" : "+v"(x))
#define BAR()                 asm volatile("s_waitcnt lgkmcnt(0)\n\ts_barrier" ::: "memory")
#define BAR_RAW()             asm volatile("s_barrier" ::: "memory")
#define SCHED()               __builtin_amdgcn_sched_barrier(0)

DEV u32x4 tr_frag(const char* p0, const char* p1) {
  u32x2 a = __builtin_bit_cast(u32x2, __builtin_amdgcn_ds_read_tr16_b64_v4i16((LAS s16x4*)p0));
  u32x2 b = __builtin_bit_cast(u32x2, __builtin_amdgcn_ds_read_tr16_b64_v4i16((LAS s16x4*)p1));
  return (u32x4){a[0], a[1], b[0], b[1]};
}

DEV u32x4 trf(u32 a, int o0, int o1) {
  u32x2 x = __builtin_bit_cast(u32x2, __builtin_amdgcn_ds_read_tr16_b64_v4i16((LAS s16x4*)(a + o0)));
  u32x2 y = __builtin_bit_cast(u32x2, __builtin_amdgcn_ds_read_tr16_b64_v4i16((LAS s16x4*)(a + o1)));
  return (u32x4){x[0], x[1], y[0], y[1]};
}

template <int I, int N, class F> DEV void static_for(F&& f) {
  if constexpr (I < N) { f(std::integral_constant<int, I>{}); static_for<I + 1, N>(f); }
}

DEV float smul(float a, float b) { float r; asm("v_mul_f32 %0, %1, %2" : "=v"(r) : "v"(a), "v"(b)); return r; }
DEV float sadd(float a, float b) { float r; asm("v_add_f32 %0, %1, %2" : "=v"(r) : "v"(a), "v"(b)); return r; }
DEV float sfma(float a, float b, float c) { float r; asm("v_fma_f32 %0, %1, %2, %3" : "=v"(r) : "v"(a), "v"(b), "v"(c)); return r; }

struct Params { const u16 *x, *lar, *aim, *cr, *ci, *ldt, *dsk, *w1, *w3, *w2, *c, *wm; char* ws; u16* out; };
struct cm { f32x2 b, bp; };
DEV cm mk_cm(float r, float i) { cm m; m.b = (f32x2){r, i}; m.bp = (f32x2){-i, r}; return m; }
DEV f32x2 cmul(f32x2 a, cm m) { f32x2 ax = {a[0], a[0]}, ay = {a[1], a[1]}; return __builtin_elementwise_fma(ay, m.bp, ax * m.b); }
DEV u32 stat_payload(float s1, float s2) {
  const float m = s1 * (1.f / D_);
  const float var = fmaxf(s2 * (1.f / D_) - m * m, 0.f);
  return pack2(m, __builtin_amdgcn_sqrtf(rb(rb(var) + 1e-5f)));
}
DEV void poll_eq14(char* ws, u32 off) {
#pragma nounroll
  for (int sp = 0; sp < SPIN_CAP; sp++) {
    u32 v; asm volatile("global_load_dword %0, %1, %2 sc1\n\ts_waitcnt vmcnt(0)" : "=&v"(v) : "v"(off), "s"(ws) : "memory");
    if (__all(v == 14u)) break;
    __builtin_amdgcn_s_sleep(2);
  }
}
#define PSLEEP 2
#define PSLEEP_H 8
template <int SL> DEV void poll_s2(char* ws, int xcd, int lane) {
  const u32 off = OFF_CTR + (u32)(xcd * 16 + (lane & 15)) * 128;
  u32 v = lane < 16 ? 0u : 14u;
#pragma nounroll
  for (int sp = 0; sp < SPIN_CAP; sp++) {
    if (v != 14u) asm volatile("global_load_dword %0, %1, %2 sc1\n\ts_waitcnt vmcnt(0)" : "+v"(v) : "v"(off), "s"(ws) : "memory");
    if (__all(v == 14u)) break;
    __builtin_amdgcn_s_sleep(SL);
  }
}
DEV f32x2 up2(u32 u) { return (f32x2){lo_f(u), hi_f(u)}; }
DEV u32 pk2(f32x2 v) { return pack2(v[0], v[1]); }
#define DMA64(addr, lds)  asm volatile("s_mov_b32 m0, %1\n\ts_nop 0\n\tglobal_load_lds_dwordx4 %0, off" :: "v"(addr), "s"(lds) : "memory")
constexpr int rpos(int r) { return (r < 8 ? r : (r ^ 4)) * 272; }
template <int R0> DEV void wrt8(u32 m0v, const u32 (&v)[8]) {
  asm volatile("s_mov_b32 m0, %8\n\ts_nop 0\n\t"
               "ds_write_addtid_b32 %0 offset:%9\n\tds_write_addtid_b32 %1 offset:%10\n\tds_write_addtid_b32 %2 offset:%11\n\tds_write_addtid_b32 %3 offset:%12\n\t"
               "ds_write_addtid_b32 %4 offset:%13\n\tds_write_addtid_b32 %5 offset:%14\n\tds_write_addtid_b32 %6 offset:%15\n\tds_write_addtid_b32 %7 offset:%16"
               :: "v"(v[0]), "v"(v[1]), "v"(v[2]), "v"(v[3]), "v"(v[4]), "v"(v[5]), "v"(v[6]), "v"(v[7]), "s"(m0v),
                  "i"(rpos(R0)), "i"(rpos(R0 + 1)), "i"(rpos(R0 + 2)), "i"(rpos(R0 + 3)), "i"(rpos(R0 + 4)), "i"(rpos(R0 + 5)), "i"(rpos(R0 + 6)), "i"(rpos(R0 + 7)) : "memory");
}
DEV u32 pkh(f32x2 v) { return packh(v[0], v[1]); }
DEV int qidx(int l) {
  u32 bits = pack2((float)l, 0.f) & 0xffffu;
  int q = l < 128 ? l : (int)bits - 0x4300 + 128;
  return l < 0 ? 641 : q;
}

DEV void lds_arrive(char* smem, int idx, int lane) {
  const u32 a = (u32)(size_t)smem + LDS_CNT + 4 * idx;
  asm volatile("s_waitcnt lgkmcnt(0)\n\ts_mov_b64 exec, 1\n\tds_add_u32 %0, %1\n\ts_mov_b64 exec, -1" :: "v"(a), "v"(1u) : "memory");
}
DEV void lds_post(char* smem, int idx) {
  const u32 a = (u32)(size_t)smem + LDS_CNT + 4 * idx;
  asm volatile("s_mov_b64 exec, 1\n\tds_add_u32 %0, %1\n\ts_mov_b64 exec, -1" :: "v"(a), "v"(1u) : "memory");
}
DEV u32 lds_peek(u32 a) { u32 v; asm volatile("ds_read_b32 %0, %1\n\ts_waitcnt lgkmcnt(0)" : "=v"(v) : "v"(a) : "memory"); return rfl(v); }
DEV void lds_wait(char* smem, int idx, u32 n) {
  const u32 a = (u32)(size_t)smem + LDS_CNT + 4 * idx;
#pragma nounroll
  for (int sp = 0; sp < (1 << 22); sp++) {
    u32 v; asm volatile("ds_read_b32 %0, %1\n\ts_waitcnt lgkmcnt(0)" : "=v"(v) : "v"(a) : "memory");
    if (rfl(v) >= n) break;
    __builtin_amdgcn_s_sleep(1);
  }
}

constexpr int pp0_ops(int s) { return ((s & 1) == 0 && s <= 18 ? 1 : 0) + ((s & 1) == 1 && s >= 5 && s <= 25 ? 1 : 0) + (s == 20 ? 2 : 0); }
constexpr int pp0_sum(int a, int b) { int n = 0; for (int s = a; s < b; s++) n += pp0_ops(s); return n; }
constexpr int pp0_wait(int c) { return c <= 3 ? (3 - c) + pp0_sum(0, 4 * c - 2) : pp0_sum(2 * (c - 4) + 1, 4 * c - 2); }
constexpr int LDS_ST2 = 155648;
constexpr int LDS_W2 = 114688;

__global__ __launch_bounds__(512, 2) void fused_kernel(Params p) {
  __shared__ __attribute__((aligned(16))) char smem[LDS_TOTAL];
  const int t = threadIdx.x, lane = t & 63;
  const int w = rfl(t >> 6);
  const int b = blockIdx.x, xcd = b & 7, bi = b >> 3;
  const bool is_main = bi < 28;
  char* const ws = p.ws;
  const u32 lbase = (u32)(size_t)smem;
  STAMP_DECL;
  STAMP(0);

  asm volatile("" :: "s"(p.x), "s"(p.w2), "s"(p.ws));
  uint64_t pc0; asm volatile("s_getpc_b64 %0" : "=s"(pc0));
  const char* const code0 = (const char*)(pc0 & ~(uint64_t)127);
  STAMP(9);
  if (t < 32 && (t < 4 || t >= 12)) *(u32*)(smem + LDS_CNT + 4 * t) = 0;
  const int row = 8 * b + w;
  const int pid = xcd * 4 + (bi - 28);
  const int pq = pid < 28 ? pid : 0;
  const int wk = w < 7 ? w : 6;
  constexpr int PST = 46080, PSW = 16384;
  const u32 lst = lbase + PST + wk * PSW;
  auto row_dma = [&]() {
  if (lane < 57) {
    const uint64_t a = lane < 56 ? (uint64_t)p.x + (size_t)row * 896 + lane * 16 : (uint64_t)ws;
    const u32 l_ = lbase + LDS_TX + w * 912;
    asm volatile("s_mov_b32 m0, %1\n\ts_nop 0\n\tglobal_load_lds_dwordx4 %0, off sc1" :: "v"(a), "s"(l_) : "memory");
  }
  };
  const u32 wl_row = lane >> 3, wl_col = ((lane & 7) ^ ((lane >> 2) & 4)) * 16;
  const int mu1 = pid < 21 ? pid : (pid < 28 ? pid + 7 : pid - 7);
  const u32 o_m1 = (u32)(64 * wk + wl_row) * (6 * D_ * 2) + mu1 * 128 + wl_col;
  int bi_early = bi; asm volatile("" : "+s"(bi_early));
  if (bi_early >= 28) {
    row_dma();
    { u32 o = (lane < 56 ? lane : 55) * 16, l_ = lbase + 16384 + w * 1024; DMA(o, p.c, l_); }
  }

  auto get_tag = [&]() { return rfl(*(const u32*)(smem + LDS_TX + w * 912 + 896)) * 1664525u + 1013904223u; };
  auto ln_publish = [&](u32 tag, int tn) {
    u32x4 xr = *(const u32x4*)(smem + LDS_TX + w * 912 + (lane < 56 ? lane : 55) * 16);
    const u32x4 xz = lane < 56 ? xr : (u32x4){0, 0, 0, 0};
    float s1 = 0.f, s2 = 0.f, a1 = 0.f, a2 = 0.f;
    s1 = dot2(xz[0], 0x3f803f80u, s1); s2 = dot2(xz[0], xz[0], s2); a1 = dot2(xz[1], 0x3f803f80u, a1); a2 = dot2(xz[1], xz[1], a2);
    s1 = dot2(xz[2], 0x3f803f80u, s1); s2 = dot2(xz[2], xz[2], s2); a1 = dot2(xz[3], 0x3f803f80u, a1); a2 = dot2(xz[3], xz[3], a2);
    float r = red32(s1 + a1, s2 + a2);
    r += dppf<0x111, 0xf>(r); r += dppf<0x112, 0xf>(r); r += dppf<0x114, 0xf>(r); r += dppf<0x118, 0xf>(r); r += dppf<0x142, 0xa>(r);
    const float S = __int_as_float(__builtin_amdgcn_readlane(__float_as_int(r), 31)), Q = __int_as_float(__builtin_amdgcn_readlane(__float_as_int(r), 63));
    const u32 pay = stat_payload(S, Q);
    const float m = lo_f(pay), rs = __builtin_amdgcn_rcpf(hi_f(pay));
#pragma unroll
    for (int q = 0; q < 4; q++) xr[q] = pk2((up2(xr[q]) - m) * rs);
    *(u32x4*)(smem + tn + w * 912 + (lane < 56 ? lane : 56) * 16) = xr;
    if (w == 0) WAITV(1);
    BAR();
    const int j = 112 * w + (lane < 56 ? lane : 55);
    const u32* src = (const u32*)(smem + LDS_TX) + (j & 3) * 456 + (j >> 2);
    const u32* srn = (const u32*)(smem + tn) + (j & 3) * 456 + (j >> 2);
    const u32 o = (u32)(j >> 2) * (L_ * 8) + b * 64 + (j & 3) * 16;
    u32x4 n0 = {srn[0], tag, srn[228], tag}, n1 = {srn[14], tag, srn[242], tag};
    u32x4 v0 = {src[0], tag, src[228], tag}, v1 = {src[14], tag, src[242], tag};
    ST16S(o, n0, ws + OFF_NG); ST16S(o, n1, ws + OFF_NG + 14 * (L_ * 8));
    ST16S(o, v0, ws + OFF_XG); ST16S(o, v1, ws + OFF_XG + 14 * (L_ * 8));
    if (t == 0) { u32 oo = OFF_STG + b * 64; ST4S(oo, tag, ws); }
  };

  auto ln_publish0 = [&](u32 tag) {
    BAR();
    const int j = 112 * w + (lane < 56 ? lane : 55);
    const u32* src = (const u32*)(smem + LDS_TX) + (j & 3) * 456 + (j >> 2);
    const u32 o = OFF_XG + (u32)(j >> 2) * (L_ * 8) + b * 64 + (j & 3) * 16;
    u32x4 v0 = {src[0], tag, src[228], tag}, v1 = {src[14], tag, src[242], tag};
    u32x4 xr = *(const u32x4*)(smem + LDS_TX + w * 912 + (lane < 56 ? lane : 55) * 16);
    ST16S(o, v0, ws); ST16S(o, v1, ws + 14 * (L_ * 8));
    if (lane >= 56) xr = (u32x4){0, 0, 0, 0};
    float s1 = 0.f, s2 = 0.f, a1 = 0.f, a2 = 0.f;
    s1 = dot2(xr[0], 0x3f803f80u, s1); s2 = dot2(xr[0], xr[0], s2); a1 = dot2(xr[1], 0x3f803f80u, a1); a2 = dot2(xr[1], xr[1], a2);
    s1 = dot2(xr[2], 0x3f803f80u, s1); s2 = dot2(xr[2], xr[2], s2); a1 = dot2(xr[3], 0x3f803f80u, a1); a2 = dot2(xr[3], xr[3], a2);
    float r = red32(s1 + a1, s2 + a2);
    r += dppf<0x111, 0xf>(r); r += dppf<0x112, 0xf>(r); r += dppf<0x114, 0xf>(r); r += dppf<0x118, 0xf>(r); r += dppf<0x142, 0xa>(r);
    const float S = __int_as_float(__builtin_amdgcn_readlane(__float_as_int(r), 31)), Q = __int_as_float(__builtin_amdgcn_readlane(__float_as_int(r), 63));
    const u32 pay = stat_payload(S, Q);
    if (w == 0) WAITV(2);
    u32 old;
    { const u32 a = lbase + LDS_CNT + 96 + 4 * w, c = lbase + LDS_CNT + 4 * 0;
      asm volatile("s_mov_b64 exec, 1\n\tds_write_b32 %1, %2\n\tds_add_rtn_u32 %0, %3, %4\n\ts_mov_b64 exec, -1\n\ts_waitcnt lgkmcnt(0)" : "=&v"(old) : "v"(a), "v"(pay), "v"(c), "v"(1u) : "memory"); }
    if (rfl(old) == 7u) {
      u32x2 g = {*(const u32*)(smem + LDS_CNT + 96 + 4 * (lane & 7)), tag};
      u32 oo = OFF_STR + (lane >> 3) * 16384 + (8 * b + (lane & 7)) * 8; ST8S(oo, g, ws);
    }
  };

  if (__builtin_expect(!is_main, 0)) {
    STAMP(15);
    WAITV(0);
    STAMP(24);
    STAMP(17);
    const u32 tag = get_tag();
    if (OPT_NG) ln_publish(tag, 16384); else ln_publish0(tag);
    STAMP(1);
    const int mu2 = pid < 3 ? 25 + pid : 32 + pid;
    const u32 o_m2 = (u32)(64 * wk + wl_row) * (6 * D_ * 2) + (pid < 10 ? mu2 : 0) * 128 + wl_col;
    if (w < 7) {
#pragma unroll
      for (int i = 0; i < 8; i++) { const char* bs = (const char*)p.wm + (size_t)i * 8 * (6 * D_ * 2); u32 l_ = lst + i * 1024; DMA(o_m1, bs, l_); }
    }
    u32* vec = (u32*)(smem);
    float* red = (float*)(smem + 2048);
    if (lane < 32) {
      const u32 craw = *(const u32*)(smem + 16384 + w * 1024 + (32 * wk + lane) * 4);
      float a = lo_f(craw), c2 = hi_f(craw);
      vec[32 * wk + lane] = pack2(a / (1.f + fexp(-a)), c2 / (1.f + fexp(-c2)));
    }
    __builtin_amdgcn_wave_barrier();
    STAMP(16);
    const char* crd0 = smem + PST + wk * PSW + (lane >> 5) * 1024 + ((lane & 15) >> 2) * 128 + ((lane >> 4) & 1) * 32 + (lane & 3) * 8;
    const char* crd[2] = {crd0 + ((lane >> 3) & 1) * 64, crd0 + (((lane >> 3) & 1) ^ 1) * 64};
    u32x4 A[4];
#pragma unroll
    for (int i = 0; i < 4; i++) A[i] = *(const u32x4*)((const char*)vec + (4 * wk + i) * 32 + (lane >> 5) * 16);
    u32 j2o = 0; const char* j2b = (const char*)p.wm; int j2s = 0;
    auto mod_unit = [&](int mu, int bo) {
#pragma unroll
      for (int h = 0; h < 2; h++) {
        f32x16 a2 = {};
#pragma unroll
        for (int i = 0; i < 4; i++) a2 = __builtin_amdgcn_mfma_f32_32x32x16_bf16(asb(A[i]), asb(tr_frag(crd[h] + bo + i * 2048, crd[h] + bo + i * 2048 + 512)), a2, 0, 0, 0);
        if (lane < 32) red[w * 256 + h * 32 + lane] = a2[0];
      }
      BAR();
      if (w == 7 && lane < 32) {
        f32x2 s = {0.f, 0.f};
#pragma unroll
        for (int i = 0; i < 7; i++) s += *(const f32x2*)(red + i * 256 + 2 * lane);
        u32x2 gr = {pk2(s), tag};
        u32 oo = OFF_MODG + (u32)(32 * mu + lane) * 8; ST8S(oo, gr, ws);
      }
      BAR();
    };
    if (w < 7) {
      const u32 o2 = pid < 4 ? o_m2 : (u32)(64 * (pid - 4) + wl_row) * (D_ * 2) + w * 128 + wl_col;
      const char* b2 = pid < 4 ? (const char*)p.wm : (const char*)p.w2;
      const int st2_ = pid < 4 ? 8 * (6 * D_ * 2) : 8 * (D_ * 2);
      WAITV(0);
#pragma unroll
      for (int i = 0; i < 4; i++) { const char* bs = b2 + i * st2_; u32 l_ = lst + 8192 + i * 1024; DMA(o2, bs, l_); }
      j2o = o2; j2b = b2 + 4 * st2_; j2s = st2_;
    }
#pragma nounroll
    for (int rnd = 0; rnd < (pid < 4 ? 2 : 1); rnd++) {
      if (rnd == 1) WAITV(0);
      mod_unit(rnd == 0 ? mu1 : mu2, rnd * 8192);
      if (rnd == 0 && w < 7) {
#pragma unroll
        for (int i = 0; i < 4; i++) { const char* bs = j2b + i * j2s; u32 l_ = lst + 12288 + i * 1024; DMA(j2o, bs, l_); }
      }
    }
    {
      STAMP(2);
      if (w < 7 && pid >= 4) {
        WAITV(0);
        const u32 oo = OFF_W2T + (u32)((2 * w * 112 + 4 * (pid - 4)) * 64 + lane) * 16;
#pragma unroll
        for (int i = 0; i < 8; i++) { u32x4 f = tr_frag(crd[i >> 2] + 8192 + (i & 3) * 2048, crd[i >> 2] + 8192 + (i & 3) * 2048 + 512); const char* bs = ws + ((i >> 2) * 112 + (i & 3)) * 1024; ST16S(oo, f, bs); }
      }
      {
        u32 g0; const u32 og = (u32)(32 * w + (lane & 31)) * 64 + OFF_STR + xcd * 16384 + 4;
#pragma nounroll
        for (int sp = 0; sp < SPIN_CAP; sp++) {
          asm volatile("global_load_dword %0, %1, %2 sc1\n\ts_waitcnt vmcnt(0)" : "=&v"(g0) : "v"(og), "s"(ws) : "memory");
          if (__all(g0 == tag)) break;
          __builtin_amdgcn_s_sleep(16);
        }
      }
      BAR();
      if (w == 7) {
        u32 old = 0;
        if (lane == 0) old = __hip_atomic_fetch_add((u32*)(ws + OFF_CTR + 176 * 128), 1u, __ATOMIC_RELAXED, __HIP_MEMORY_SCOPE_AGENT);
        if (rfl(old) == 31u && lane < 8) { u32 of = OFF_W2F + lane * 128; ST4S(of, tag, ws); }
      }
      STAMP(6);
      {
        const int cj = (xcd >> 1) * 4 + (bi - 28);
        const int fg = (xcd & 1) * 14 + (cj < 14 ? cj : 0);
        u32x2 gt; const u32 o_gt = OFF_MODG + (u32)(3 * 224 + (t < 448 ? t : 447)) * 8;
#pragma nounroll
        for (int sp = 0; sp < SPIN_CAP; sp++) {
          asm volatile("global_load_dwordx2 %0, %1, %2 sc1\n\ts_waitcnt vmcnt(0)" : "=&v"(gt) : "v"(o_gt), "s"(ws) : "memory");
          if (__all(gt[1] == tag)) break;
          __builtin_amdgcn_s_sleep(16);
        }
        if (w < 7) {
          const u32 o = (u32)(64 * wk + wl_row) * (F_ * 2) + fg * 128 + wl_col;
#pragma unroll
          for (int i = 0; i < 16; i++) { const char* bs = (const char*)(i < 8 ? p.w1 : p.w3) + (size_t)(i & 7) * 8 * (F_ * 2); u32 l_ = lst + i * 1024; DMA(o, bs, l_); }
        }
        if (t < 448) {
          u32 v = gt[0];
          if (t >= 224) v = pack2(1.f + lo_f(v), 1.f + hi_f(v));
          vec[t] = v;
        }
        BAR();
        u32x4 A2[4];
#pragma unroll
        for (int i = 0; i < 4; i++) A2[i] = *(const u32x4*)((const char*)vec + ((lane & 1) ? 0 : 896) + (4 * wk + i) * 32 + (lane >> 5) * 16);
#pragma unroll
        for (int g = 0; g < 4; g++) {
          if (g == 0) WAITV(8);
          if (g == 2) WAITV(0);
          f32x16 acc = {};
#pragma unroll
          for (int i = 0; i < 4; i++) acc = __builtin_amdgcn_mfma_f32_32x32x16_bf16(asb(A2[i]), asb(tr_frag(crd[g & 1] + (g >> 1) * 8192 + i * 2048, crd[g & 1] + (g >> 1) * 8192 + i * 2048 + 512)), acc, 0, 0, 0);
          if (lane < 32) { red[w * 256 + g * 64 + lane] = acc[0]; red[w * 256 + g * 64 + 32 + lane] = acc[1]; }
        }
        BAR();
        if (t < 256 && cj < 14) {
          float s = 0.f;
#pragma unroll
          for (int i = 0; i < 7; i++) s += red[i * 256 + t];
          u32x2 gr = {__float_as_uint(s), tag};
          u32 oo = OFF_CSG + (u32)(((t >> 5) & 1) * 3584 + (t >> 7) * F_ + fg * 64 + ((t >> 6) & 1) * 32 + (t & 31)) * 8; ST8S(oo, gr, ws);
        }
      }
      if (w < 7 && pid >= 4 && pid < 10) {
#pragma unroll
        for (int i = 0; i < 8; i++) { const char* bs = (const char*)p.wm + (size_t)i * 8 * (6 * D_ * 2); u32 l_ = lst + 8192 + i * 1024; DMA(o_m2, bs, l_); }
      }
      STAMP(3);
      if (w == 7) poll_s2<PSLEEP_H>(ws, xcd, lane);
      BAR();
      STAMP(4);
    {
      const u32 o = OFF_X1T + (u32)(64 * wk + wl_row) * (L_ * 2) + pid * 128 + wl_col;
#pragma unroll
      for (int i = 0; i < 8; i++) { const char* bs = ws + (size_t)i * 8 * (L_ * 2); u32 l_ = lst + i * 1024; if (w < 7) DMAS(o, bs, l_); }
      float s1[2][2] = {}, s2[2][2] = {};
#pragma unroll
      for (int i = 0; i < 4; i++) {
        WAITV(6 - 2 * i);
#pragma unroll
        for (int h = 0; h < 2; h++) {
          const u32x4 f = tr_frag(crd[h] + i * 2048, crd[h] + i * 2048 + 512);
#pragma unroll
          for (int q = 0; q < 4; q++) { const u32 u = f[q]; s1[h][q & 1] = dot2(u, 0x3f803f80u, s1[h][q & 1]); s2[h][q & 1] = dot2(u, u, s2[h][q & 1]); }
        }
      }
      float* rd2 = (float*)smem;
#pragma unroll
      for (int h = 0; h < 2; h++) rd2[w * 128 + (lane >> 5) * 64 + h * 32 + (lane & 31)] = red32(s1[h][0] + s1[h][1], s2[h][0] + s2[h][1]);
      BAR();
      if (t < 64) {
        float S = 0.f, Q = 0.f;
#pragma unroll
        for (int i = 0; i < 7; i++) { S += rd2[i * 128 + t]; Q += rd2[i * 128 + 64 + t]; }
        u32x2 gr = {stat_payload(S, Q), tag};
        u32 oo = OFF_ST2G + (pid * 64 + t) * 8; ST8S(oo, gr, ws);
      }
    }
      STAMP(5);
      if (pid >= 4 && pid < 10) { BAR(); mod_unit(mu2, 8192); }
    }
    STAMP_OUT;
    return;
  }

  const int id = xcd * 28 + bi;
  const int pm = (xcd >> 1) * 4 + (bi & 3), cg = (xcd & 1) * 7 + (bi >> 2);
  const int kch = w >> 2, tt = t & 255;
  u32 r_lar, r_aim, r_cr, r_ci, r_ldt;
  {
    u32 o = (u32)((2 * id + kch) * N_ + tt) * 2;
    LD2(r_lar, o, p.lar); LD2(r_aim, o, p.aim); LD2(r_cr, o, p.cr); LD2(r_ci, o, p.ci);
    u32 o2 = (2 * id + kch) * 2; LD2(r_ldt, o2, p.ldt);
  }
  row_dma();
  {
    u32 oc = (u32)(2 * (8 * bi + w) + (lane & 1)) * 128; oc = (OPT_CODEPF && oc < CODE_BYTES) ? oc : 0u;
    const u32 l_ = lbase + LDS_JUNK;
    asm volatile("s_mov_b32 m0, %2\n\ts_nop 0\n\tglobal_load_lds_dword %0, %1" :: "v"(oc), "s"(code0), "s"(l_) : "memory");
  }
  if (t == 0) { u32 o = OFF_CTR + b * 128; ST4S(o, 0u, ws); }
  STAMP(15);
  const char* const wbase = (const char*)((w & 1) ? p.w3 : p.w1) + (size_t)(8 * (w >> 2)) * (F_ * 2) + (128 * cg + 64 * ((w >> 1) & 1)) * 2;
  const u32 wlane = (u32)(lane >> 3) * (F_ * 2) + ((((lane >> 1) & 3) ^ (2 * (w & 1) + ((lane >> 4) & 1))) * 2 + (lane & 1)) * 16;
  const int krot = OPT_ROT == 1 ? (7 * (bi & 3) + 4 * (bi >> 2)) % 28 : (OPT_ROT == 2 ? 7 * (xcd >> 1) : 0);
#define KS(s) ((s) + krot >= 28 ? (s) + krot - 28 : (s) + krot)
#define DMA_W2(s, drow, ldsoff) do { const char* a_ = wbase + (int64_t)(16 * KS(s) + (drow)) * (F_ * 2); u32 l_ = lbase + (ldsoff) + w * 1024; DMA(wlane, a_, l_); } while (0)
#define DMA_W(s, ldsoff) DMA_W2(s, 0, ldsoff)
  u32 gix[6];
#pragma unroll
  for (int q = 0; q < 6; q++) { gix[q] = (u32)(2 * qidx(8 * tt - 4 + 2 * q)) | ((u32)(2 * qidx(8 * tt - 3 + 2 * q)) << 16); PIN(gix[q]); }
  STAMP(24);
  u32 tag;
  auto do_ln = [&]() {
    if (w == 0) WAITV(2); else WAITV(1);
    STAMP(25);
    tag = get_tag();
    ln_publish0(tag);
#pragma unroll
    for (int s = 0; s < N0; s++) DMA_W(s, LDS_S1 + s * 8192);
    lds_post(smem, 14);
    STAMP(1);
  };
  if (OPT_LNPOS == 0) do_ln();
  if (w != 0) WAITV(OPT_LNPOS ? 2 : 4);
  asm volatile("s_waitcnt vmcnt(%5)" : "+v"(r_lar), "+v"(r_aim), "+v"(r_cr), "+v"(r_ci), "+v"(r_ldt) : "i"(OPT_LNPOS ? 3 : 4) : "memory");
  u32x4 W[28];
  const u32 wrd = lbase + (lane >> 5) * 4096 + (w >> 2) * 2048 + ((lane >> 4) & 1) * 1024 + ((lane & 15) >> 2) * 128
                  + ((w & 3) ^ ((lane >> 3) & 3)) * 32 + (((lane & 1) << 1) | ((lane >> 1) & 1)) * 8;
  {
    const float dt = rb(fexp(lo_f(r_ldt)));
    const float a2 = -rb(fexp(lo_f(r_lar))) * dt * 1.44269504088896341f;
    const float bb = lo_f(r_aim) * dt;
    const float bh = bb * 0.15915494309189535f;
    const float bl = __builtin_fmaf(bb, 0.15915494309189535f, -bh) + bb * 6.4206382e-9f;
    auto zpow = [&](float pw) {
      float m = __builtin_amdgcn_exp2f(a2 * pw);
      float th = __builtin_amdgcn_fractf(bh * pw) + bl * pw;
      return (f32x2){m * __builtin_amdgcn_cosf(th), m * __builtin_amdgcn_sinf(th)};
    };
    const f32x2 z1v = zpow(1.f), z8v = zpow(8.f), z128v = zpow(128.f);
    const cm z1 = mk_cm(z1v[0], z1v[1]), z8c = mk_cm(z8v[0], -z8v[1]), z128 = mk_cm(z128v[0], z128v[1]);
    const float c_r = lo_f(r_cr), c_i = lo_f(r_ci);
    const u32 ar_ = r_cr & 0x7fffu, ai_ = r_ci & 0x7fffu;
    int ex = (int)(wave_maxu(ar_ > ai_ ? ar_ : ai_) >> 7) - 126;
    ex = ex < -90 ? -90 : ex;
    if (lane == 0) *(float*)(smem + LDS_CNT + 16 + 4 * w) = __builtin_amdgcn_ldexpf(2.f * dt, ex - 28);
    if (OPT_LNPOS == 1) do_ln();
    char* buf = smem + LDS_KG + w * KGW;
    const u32 m0v = lbase + LDS_KG + w * KGW;
    const int c = lane & 15, g = lane >> 4;
    const char* rd = buf + (c < 8 ? c : (c ^ 4)) * 272 + ((g & 1) * 8 + (g >> 1) * 4) * 16;
    f32x2 x[8];
    u32 v[8];
    x[0] = (f32x2){__builtin_amdgcn_ldexpf(c_r, 14 - ex), __builtin_amdgcn_ldexpf(c_i, 14 - ex)};
    f32x2 y = {16384.f, 0.f};
    v[0] = packh(16384.f, 0.f);
#pragma unroll
    for (int i = 1; i < 8; i++) { y = cmul(y, z8c); v[i] = pkh(y); x[i] = cmul(x[i - 1], z1); }
    wrt8<0>(m0v, v);
    if (OPT_LNPOS == 2) do_ln();
    u32 v2[8], v3[8];
#pragma unroll
    for (int i = 0; i < 8; i++) { y = cmul(y, z8c); v[i] = pkh(y); v2[i] = pkh(x[i]); x[i] = cmul(x[i], z128); v3[i] = pkh(x[i]); }
    wrt8<8>(m0v, v);
    u32x4 bf[4], af[4];
#pragma unroll
    for (int s = 0; s < 4; s++) bf[s] = *(const u32x4*)(rd + 16 * s);
    wrt8<0>(m0v, v2); wrt8<8>(m0v, v3);
    if (OPT_LNPOS == 3) do_ln();
    f32x4 acc[3];
    auto rdA = [&]() {
#pragma unroll
      for (int s = 0; s < 4; s++) af[s] = *(const u32x4*)(rd + 16 * s);
    };
    auto mm = [&](int T) {
      f32x4 a = {0, 0, 0, 0};
#pragma unroll
      for (int s = 0; s < 4; s++) a = __builtin_amdgcn_mfma_f32_16x16x32_f16(ash(af[s]), ash(bf[s]), a, 0, 0, 0);
      acc[T] = a;
    };
    rdA();
#pragma unroll
    for (int r = 0; r < 2; r++)
#pragma unroll
      for (int i = 0; i < 4; i++) { x[2 * i] = cmul(x[2 * i], z128); v[4 * r + i] = pkh(x[2 * i]); }
#pragma unroll
    for (int r = 0; r < 4; r++)
#pragma unroll
      for (int i = 0; i < 2; i++) { x[4 * i] = cmul(x[4 * i], z128); v2[2 * r + i] = pkh(x[4 * i]); }
    mm(0);
    wrt8<0>(m0v, v); wrt8<8>(m0v, v2);
    rdA();
    {
      const f32x2 q = cmul(z128v, z128);
      const cm z256 = mk_cm(q[0], q[1]);
      f32x2 e = cmul(x[0], z128), o = cmul(x[0], z256);
      v[0] = pkh(e); v[1] = pkh(o);
#pragma unroll
      for (int r = 1; r < 4; r++) { e = cmul(e, z256); o = cmul(o, z256); v[2 * r] = pkh(e); v[2 * r + 1] = pkh(o); }
      e = cmul(e, z256);
      mm(1);
      wrt8<0>(m0v, v);
      *(u32*)(buf + rpos(8) + lane * 4) = pkh(e);
    }
    __builtin_amdgcn_wave_barrier();
    rdA();
    __builtin_amdgcn_wave_barrier();
    mm(2);
    float* tab = (float*)buf;
    *(f32x4*)(tab + 4 * (g & 1) + 128 * (g >> 1) + 8 * c) = acc[0];
    const int q1 = g < 2 ? 256 + 64 * g + 4 * c : 384 + 64 * (g - 2) + 2 * c, q1d = g < 2 ? 2 : 32;
    *(f32x2*)(tab + q1) = (f32x2){acc[1][0], acc[1][1]};
    *(f32x2*)(tab + q1 + q1d) = (f32x2){acc[1][2], acc[1][3]};
#pragma unroll
    for (int r = 0; r < 4; r++) tab[512 + 16 * (4 * g + r) + c] = acc[2][r];
  }
  STAMP(2);
  auto finish_k = [&](auto mid) {
    lds_wait(smem, 18 + kch, 4);
    if (OPT_NG) lds_wait(smem, 14, 8);
    if (tt < 161) {
      const char* pp = smem + LDS_KG + kch * 4 * KGW + tt * 16;
      const f32x4 sc = *(const f32x4*)(smem + LDS_CNT + 16 + kch * 16);
      f32x4 v = *(const f32x4*)pp * sc[0];
#pragma unroll
      for (int i = 1; i < 4; i++) v = __builtin_elementwise_fma(*(const f32x4*)(pp + i * KGW), (f32x4){sc[i], sc[i], sc[i], sc[i]}, v);
      if (tt == 160) v[1] = 0.f;
      *(u32x2*)(smem + LDS_T16 + kch * 1408 + tt * 8) = (u32x2){pack2(v[0], v[1]), pack2(v[2], v[3])};
    } else if (tt >= 208) {
      const int z = tt - 208;
      const int zo = z < 32 ? LDS_HP + kch * HP_BYTES + ((z >> 4) * 144 + (z & 15)) * 16 : LDS_KC + kch * KC_BYTES + ((z >> 2) & 3) * 4160 + (z & 3) * 16;
      *(u32x4*)(smem + zo) = (u32x4){0, 0, 0, 0};
    }
    lds_post(smem, 20 + kch);
    lds_wait(smem, 20 + kch, 4);
    mid();
    const char* t16 = smem + LDS_T16 + kch * 1408;
    u32 wv[6];
#pragma unroll
    for (int q = 0; q < 6; q++) wv[q] = (u32)*(const u16*)(t16 + (gix[q] & 0xffffu)) | ((u32)*(const u16*)(t16 + (gix[q] >> 16)) << 16);
    u32 al[5];
#pragma unroll
    for (int i = 0; i < 5; i++) al[i] = __builtin_amdgcn_alignbit(wv[i + 1], wv[i], 16);
    char* base = smem + LDS_KC + kch * KC_BYTES + 64 + 16 * tt;
    *(u32x4*)(base) = (u32x4){wv[2], wv[3], wv[4], wv[5]};
    *(u32x4*)(base + 4160) = (u32x4){al[1], al[2], al[3], al[4]};
    *(u32x4*)(base + 2 * 4160) = (u32x4){wv[1], wv[2], wv[3], wv[4]};
    *(u32x4*)(base + 3 * 4160) = (u32x4){al[0], al[1], al[2], al[3]};
  };
  lds_post(smem, 18 + kch);
  if (w < 4) {
    u32x4 gs[4], gx[4];
    const u32 o_g = (OPT_NG ? OFF_NG + (u32)id * (L_ * 8) : OFF_STR + xcd * 16384) + w * 4096 + lane * 16, o_xg = OFF_XG + (u32)id * (L_ * 8) + w * 4096 + lane * 16;
#define LD4X(g, off, tail) asm volatile("global_load_dwordx4 %0, %4, %5 sc1\n\tglobal_load_dwordx4 %1, %4, %5 offset:1024 sc1\n\tglobal_load_dwordx4 %2, %4, %5 offset:2048 sc1\n\tglobal_load_dwordx4 %3, %4, %5 offset:3072 sc1" tail \
      : "+v"(g[0]), "+v"(g[1]), "+v"(g[2]), "+v"(g[3]) : "v"(off), "s"(ws) : "memory")
    auto settle = [&](u32x4 (&g)[4], u32 off) {
      asm volatile("s_waitcnt vmcnt(0)" : "+v"(g[0]), "+v"(g[1]), "+v"(g[2]), "+v"(g[3]) :: "memory");
#pragma nounroll
      for (int sp = 0; sp < SPIN_CAP; sp++) {
        u32 bd = 0;
#pragma unroll
        for (int i = 0; i < 4; i++) bd |= (g[i][1] ^ tag) | (g[i][3] ^ tag);
        if (!__any(bd != 0)) break;
        __builtin_amdgcn_s_sleep(6);
        if (bd != 0) LD4X(g, off, "\n\ts_waitcnt vmcnt(0)");
      }
    };
#pragma unroll
    for (int i = 0; i < 4; i++) { gs[i] = (u32x4){0, 0, 0, 0}; gx[i] = (u32x4){0, 0, 0, 0}; }
    auto sweep = [&]() {
    LD4X(gx, o_xg, ""); LD4X(gs, o_g, "");
    if (OPT_TOUCH) {
      const char* tb = w == 0 ? (const char*)p.w1 : w == 1 ? (const char*)p.w3 : w == 2 ? (const char*)p.w2 : (const char*)p.wm;
      const u32 ot = w < 3 ? (u32)(id * 56 + (lane < 56 ? lane : 0)) * 128 : (u32)(2 * id + (lane & 1)) * (6 * D_ * 2) + (((lane >> 1) % 10) < 3 ? 25 + (lane >> 1) % 10 : 32 + (lane >> 1) % 10) * 128;
      const u32 l_ = lbase + LDS_JUNK;
      asm volatile("s_mov_b32 m0, %2\n\ts_nop 0\n\tglobal_load_lds_dword %0, %1" :: "v"(ot), "s"(tb), "s"(l_) : "memory");
    }
    };
    if (OPT_SWEEP_EARLY == 1) sweep();
    finish_k([&]() { if (OPT_SWEEP_EARLY == 0) sweep(); });
    if (OPT_SWEEP_EARLY == 2) sweep();
    STAMP(16);
    asm volatile("s_waitcnt vmcnt(%8)" : "+v"(gs[0]), "+v"(gs[1]), "+v"(gs[2]), "+v"(gs[3]), "+v"(gx[0]), "+v"(gx[1]), "+v"(gx[2]), "+v"(gx[3]) : "i"(OPT_TOUCH ? 1 : 0) : "memory");
    {
      u32 bd = 0;
#pragma unroll
      for (int i = 0; i < 4; i++) bd |= ((gs[i][1] ^ tag) | (gs[i][3] ^ tag)) | ((gx[i][1] ^ tag) | (gx[i][3] ^ tag));
      if (__builtin_expect(__any(bd != 0), 0)) { settle(gx, o_xg); settle(gs, o_g); }
    }
    if (!OPT_NG) {
#pragma unroll
      for (int i = 0; i < 4; i++)
#pragma unroll
        for (int e = 0; e < 2; e++) {
          const u32 pl = gs[i][2 * e];
          gs[i][2 * e] = pk2((up2(gx[i][2 * e]) - lo_f(pl)) * __builtin_amdgcn_rcpf(hi_f(pl)));
        }
    }
    lds_post(smem, 17);
    lds_wait(smem, 14, 8);
    STAMP(4);
    char* hb = smem + LDS_HP + (((lane >> 2) & 1) * 144 + 16 + 32 * w + (lane >> 3)) * 16 + (lane & 3) * 4;
    char* xb_ = smem + LDS_TX + w * 1024 + lane * 4;
#pragma unroll
    for (int i = 0; i < 4; i++) {
      *(u32*)(hb + i * 128) = __builtin_amdgcn_perm(gs[i][2], gs[i][0], 0x05040100u);
      *(u32*)(hb + i * 128 + HP_BYTES) = __builtin_amdgcn_perm(gs[i][2], gs[i][0], 0x07060302u);
      *(u32*)(xb_ + i * 256) = __builtin_amdgcn_perm(gx[i][2], gx[i][0], 0x05040100u);
      *(u32*)(xb_ + i * 256 + 4096) = __builtin_amdgcn_perm(gx[i][2], gx[i][0], 0x07060302u);
    }
#pragma unroll
    for (int s = N0; s < (N0 + 3 < 9 ? N0 + 3 : 9); s++) { DMA_W2(s, 0, LDS_S1 + s * 8192); DMA_W2(s, 8, LDS_S1 + s * 8192 + 4096); }
  } else {
    finish_k([]() {});
    STAMP(16);
    lds_wait(smem, 17, 4);
#pragma unroll
    for (int s = N0 + 3; s < (N0 + 6 < 9 ? N0 + 6 : 9); s++) { DMA_W2(s, 0, LDS_S1 + s * 8192); DMA_W2(s, -8, LDS_S1 + s * 8192 - 4096); }
    STAMP(4);
  }
  u32 cv_a, cv_b, cv_y;
  {
    const int ch = (w >> 1) & 1, hf = w & 1, r = lane & 15, g = lane >> 4, u0 = 4 * r + g;
    cv_a = lbase + LDS_KC + ch * KC_BYTES + (r >> 2) * 4160 + 2 * (32 + 16 * (1 + 8 * hf) - 8 * g - 4 + 4 * (r & 3));
    cv_b = lbase + LDS_HP + ch * HP_BYTES + ((g & 1) * 144 + 16 + r + (g >> 1) - 7 - 8 * hf) * 16;
    cv_y = lbase + LDS_KC + (hf * 2 + ch) * 8192 + (u0 ^ ((u0 >> 3) & 7)) * 16;
    PIN(cv_a); PIN(cv_b); PIN(cv_y);
  }
  STAMP(17);
  BAR();
  if (b == 0 && t == 0) { u32 z = 0; ST4S(z, tag, ws); }
  STAMP(5);
  u32 wrdA = wrd + LDS_S1, wrdB = wrd + LDS_S1 + 5 * 8192;
  PIN(wrdA); PIN(wrdB);
  auto rd_s1 = [&](int s) { return s < 5 ? trf(wrdA, s * 8192, s * 8192 + 512) : trf(wrdB, (s - 5) * 8192, (s - 5) * 8192 + 512); };
  auto rd_first = [&]() {
#pragma unroll
    for (int s = 0; s < R1; s++) W[s] = rd_s1(s);
#pragma unroll
    for (int s = 0; s < 5; s++) W[R1 + s] = trf(wrd, s * 8192, s * 8192 + 512);
  };
  if (w < 4) {
    f32x4 acc[8] = {};
    u32x4 A[2][8], B[2][8];
    auto ld = [&](int j, int u) {
      A[u][0] = trf(cv_a, 64 * j + 8, 64 * j);
#pragma unroll
      for (int d = 7; d >= 4; d--) B[u][d] = *(const LAS u32x4*)(cv_b + 256 * d + 32 * (3 - j));
#pragma unroll
      for (int a = 1; a < 8; a++) {
        A[u][a] = trf(cv_a, 512 * a + 64 * j + 8, 512 * a + 64 * j);
        if (a <= 4) B[u][4 - a] = *(const LAS u32x4*)(cv_b + 256 * (4 - a) + 32 * (3 - j));
      }
    };
    if (OPT_PRIO) __builtin_amdgcn_s_setprio(2);
    ld(0, 0);
    SCHED();
#pragma unroll
    for (int j = 0; j < 4; j++) {
      const int u = j & 1;
      if (j < 3) ld(j + 1, u ^ 1);
#pragma unroll
      for (int i = 0; i < 8; i++) {
#pragma unroll
        for (int nt = 7; nt >= 4; nt--)
          if (nt >= i) acc[nt] = __builtin_amdgcn_mfma_f32_16x16x32_bf16(asb(A[u][i]), asb(B[u][nt - i]), acc[nt], 0, 0, 0);
#pragma unroll
        for (int nt = 3; nt >= 0; nt--) {
          const int a = i - 7 + nt;
          if (a >= 0) acc[nt] = __builtin_amdgcn_mfma_f32_16x16x32_bf16(asb(A[u][a]), asb(B[u][nt - a]), acc[nt], 0, 0, 0);
        }
      }
#pragma unroll
      for (int i = 0; i < 36; i++) { __builtin_amdgcn_sched_group_barrier(0x8, 1, 0); __builtin_amdgcn_sched_group_barrier(0x100, 1, 0); }
      SCHED();
      if (j == 2) { lds_post(smem, 2); SCHED(); }
    }
    lds_wait(smem, 2, 8);
#pragma unroll
    for (int nt = 0; nt < 8; nt++) *(LAS f32x4*)(cv_y + nt * 1024) = acc[nt];
    if (OPT_PRIO) __builtin_amdgcn_s_setprio(0);
    STAMP(6);
    WAITV(0);
    BAR();
    STAMP(20);
    rd_first();
    lds_post(smem, 3);
    STAMP(21); STAMP(22); STAMP(7);
    lds_wait(smem, 3, 8);
    lds_wait(smem, 1, 5);
#pragma unroll
    for (int k = NRW; k < 14; k++) { DMA_W2(14 + k, 0, LDS_S1 + (k - 5) * 8192); DMA_W2(14 + k, 8, LDS_S1 + (k - 5) * 8192 + 4096); }
    if (w == 1) {
      const u32 of = OFF_W2F + xcd * 128;
#pragma nounroll
      for (int sp = 0; sp < SPIN_CAP; sp++) {
        u32 v; asm volatile("global_load_dword %0, %1, %2 sc1\n\ts_waitcnt vmcnt(0)" : "=&v"(v) : "v"(of), "s"(ws) : "memory");
        if (__all(v == tag)) break;
        __builtin_amdgcn_s_sleep(4);
      }
    }
    WAITV(0);
  } else {
    u32x2 gm2;
    const u32 o_gm2 = OFF_MODG + (u32)(((lane & 3) == 3 ? 4 : (lane & 3)) * 224 + id) * 8;
    u32 dsk2; { const u32 od = id * 4; LD4(dsk2, od, p.dsk); }
    LD8S(gm2, o_gm2, ws);
#pragma unroll
    for (int s = N0 + 6; s < R1; s++) { DMA_W2(s, 0, LDS_S1 + s * 8192); DMA_W2(s, -8, LDS_S1 + s * 8192 - 4096); }
    u32x4 xo_raw[2], no_raw[2], kk[2];
#pragma unroll
    for (int c = 0; c < 2; c++) {
      xo_raw[c] = *(const u32x4*)(smem + LDS_TX + c * 4096 + 16 * tt);
      no_raw[c] = *(const u32x4*)(smem + LDS_HP + c * HP_BYTES + ((tt & 1) * 144 + 16 + (tt >> 1)) * 16);
      kk[c] = *(const u32x4*)(smem + LDS_KC + c * KC_BYTES + 64 + 16 * tt);
    }
    lds_post(smem, 2); lds_post(smem, 16);
    lds_wait(smem, 16, 4);
#pragma unroll
    for (int s = 0; s < 5; s++) { DMA_W2(R1 + s, 0, r2slot(s)); DMA_W2(R1 + s, -8, r2slot(s) - 4096); }
    float zs[2][8];
#pragma unroll
    for (int c = 0; c < 2; c++) {
#pragma unroll
      for (int q = 0; q < 4; q++) { zs[c][2 * q] = lo_f(kk[c][q]); zs[c][2 * q + 1] = hi_f(kk[c][q]); }
#pragma unroll
      for (int j = 1; j < 8; j++) zs[c][j] = sadd(zs[c][j], zs[c][j - 1]);
    }
    float ex[2];
#pragma unroll
    for (int c = 0; c < 2; c++) {
      float r = zs[c][7];
      r += dppf<0x111, 0xf>(r); r += dppf<0x112, 0xf>(r); r += dppf<0x114, 0xf>(r); r += dppf<0x118, 0xf>(r); r += dppf<0x142, 0xa>(r); r += dppf<0x143, 0xc>(r);
      ex[c] = r - zs[c][7];
      if (lane == 63) *(float*)(smem + LDS_CNT + 96 + 16 * c + 4 * (w & 3)) = r;
    }
    lds_post(smem, 23);
    u32x4 RW[2 * NRW];
#pragma unroll
    for (int i = 0; i < 2 * NRW; i++) {
      const char* a_ = wbase + (int64_t)(16 * KS(14 + i / 2) - 8 * (i & 1)) * (F_ * 2);
      LD16(RW[i], wlane, a_);
    }
    asm volatile("s_waitcnt vmcnt(%2)" : "+v"(gm2), "+v"(dsk2) : "i"(10 + (N0 < 3 ? 2 * (3 - N0) : 0) + 2 * NRW) : "memory");
#pragma nounroll
    for (int sp = 0; sp < SPIN_CAP && !__all(gm2[1] == tag); sp++) {
      __builtin_amdgcn_s_sleep(8);
      asm volatile("global_load_dwordx2 %0, %1, %2 sc1\n\ts_waitcnt vmcnt(0)" : "=&v"(gm2) : "v"(o_gm2), "s"(ws) : "memory");
    }
    const u32 msa = __builtin_amdgcn_readlane(gm2[0], 0), mca = __builtin_amdgcn_readlane(gm2[0], 1);
    const u32 mga = __builtin_amdgcn_readlane(gm2[0], 2), mcb = __builtin_amdgcn_readlane(gm2[0], 3);
    const f32x2 sa = up2(msa), c1 = up2(pk2(up2(mca) + 1.f)), ga = up2(mga), c1b = up2(pk2(up2(mcb) + 1.f)), dk = up2(dsk2);
    lds_wait(smem, 23, 4);
    float hd[2][8];
#pragma unroll
    for (int c = 0; c < 2; c++) {
      const f32x4 wt = *(const f32x4*)(smem + LDS_CNT + 96 + c * 16);
      const int wq = w & 3;
      const float off = (ex[c] + (wq > 0 ? wt[0] : 0.f) + (wq > 1 ? wt[1] : 0.f) + (wq > 2 ? wt[2] : 0.f)) * sa[c];
      const float ka = c1[c] * dk[c], kb = sa[c] * dk[c];
#pragma unroll
      for (int j = 0; j < 8; j++) {
        zs[c][j] = sfma(zs[c][j], sa[c], off);
        hd[c][j] = sfma((j & 1) ? hi_f(no_raw[c][j >> 1]) : lo_f(no_raw[c][j >> 1]), ka, kb);
        PIN(zs[c][j]); PIN(hd[c][j]);
      }
    }
    STAMP(6);
    WAITV(2 * NRW);
    BAR();
    STAMP(20);
    const int ua = (2 * tt) ^ ((tt >> 2) & 7);
#pragma unroll
    for (int c = 0; c < 2; c++) {
      const char* yr = smem + LDS_KC + c * 8192;
      const f32x4 ya = *(const f32x4*)(yr + ua * 16) + *(const f32x4*)(yr + 16384 + ua * 16);
      const f32x4 yb = *(const f32x4*)(yr + (ua ^ 1) * 16) + *(const f32x4*)(yr + 16384 + (ua ^ 1) * 16);
      const f32x2 yv[4] = {{ya[0], ya[1]}, {ya[2], ya[3]}, {yb[0], yb[1]}, {yb[2], yb[3]}};
      u32x4 ox, os;
#pragma unroll
      for (int q = 0; q < 4; q++) {
        const f32x2 y = up2(pk2(yv[q] * c1[c] + (f32x2){zs[c][2 * q], zs[c][2 * q + 1]}));
        const f32x2 a = up2(pk2(y + (f32x2){hd[c][2 * q], hd[c][2 * q + 1]}));
        const f32x2 m = up2(pk2(a * ga[c]));
        ox[q] = pk2(up2(xo_raw[c][q]) + m);
        os[q] = pk2(up2(ox[q]) * c1b[c]);
      }
      const u32 o_x1 = (u32)(2 * id + c) * (L_ * 2) + tt * 16;
      ST16S(o_x1, os, ws + OFF_X1S); ST16S(o_x1, ox, ws + OFF_X1T);
    }
    rd_first();
    lds_post(smem, 3);
    STAMP(21);
    WAITV(0);
    STAMP(22);
    {
      u32 old; const u32 c = lbase + LDS_CNT + 4 * 1;
      asm volatile("s_mov_b64 exec, 1\n\tds_add_rtn_u32 %0, %1, %2\n\ts_mov_b64 exec, -1\n\ts_waitcnt lgkmcnt(0)" : "=&v"(old) : "v"(c), "v"(1u) : "memory");
      if (rfl(old) == 3u) { if (lane < 8) { u32 o = OFF_CTR + (u32)(lane * 16 + (id & 15)) * 128; ATOM_INC(o, ws); } lds_post(smem, 1); }
    }
    STAMP(7);
    lds_wait(smem, 3, 8);
    u32 d3 = lbase + LDS_R3 + w * 1024 + lane * 16 - 4096, d1 = lbase + LDS_S1 + w * 1024 + lane * 16 - 4096, d1b = d1 + 5 * 8192;
    PIN(d3); PIN(d1); PIN(d1b);
#pragma unroll
    for (int i = 0; i < 2 * NRW; i++) {
      const int k = i / 2, o = (i & 1) ? 0 : 4096;
      PIN(RW[i]);
      *(LAS u32x4*)(k < 5 ? d3 + k * 8192 + o : (k < 10 ? d1 + (k - 5) * 8192 + o : d1b + (k - 10) * 8192 + o)) = RW[i];
    }
  }
  const int p2 = 2 * xcd + ((bi >> 1) & 1), e2 = bi & 1, dg = bi >> 2;
  const u32 o_w2 = OFF_W2T + (u32)((2 * dg * 112 + 14 * w) * 64 + lane) * 16;
  const int rot = OPT_ROT2 == 1 ? 2 * dg : (OPT_ROT2 == 2 ? (xcd * 7) / 4 : 0);
#define KROT(s) (((s) + rot >= 14 ? (s) + rot - 14 : (s) + rot) * 1024)
  u32x4 W2[28];
#define LD_W2(i) do { const char* bs_ = ws + ((i) & 1) * 112 * 1024 + KROT((i) >> 1); LD16S(W2[i], o_w2, bs_); } while (0)
  u32 w3 = wrd + LDS_R3; PIN(w3);
  u32 o_xp = OFF_X1S + (u32)(lane >> 3) * (L_ * 2) + (128 * pm) * 2 + ((lane & 7) ^ ((lane >> 2) & 4)) * 16;
  const int xrow0 = 8 * w + 16 * krot;
#define DMA_X(i) do { int r_ = xrow0 + ((i) % 7) * 64; r_ = r_ >= 448 ? r_ - 448 : r_; const char* bs_ = ws + (size_t)r_ * (L_ * 2) + ((i) / 7) * 128; \
    u32 l_ = lbase + (i) * 8192 + w * 1024; DMAS(o_xp, bs_, l_); } while (0)
  u32 xrdA = lbase + (lane >> 5) * 1024 + ((lane & 15) >> 2) * 128 + ((lane >> 4) & 1) * 32 + (lane & 3) * 8 + ((lane >> 3) & 1) * 64;
  u32 xrdB = xrdA ^ 64;
  u32 xrdC = xrdA + 57344, xrdD = xrdB + 57344;
  u32x2 st2[4], csg;
  u32x4 Wx;
  u32 o_s2 = OFF_ST2G + (u32)(128 * pm + (lane & 31)) * 8;
  u32 o_h = OFF_H + (u32)((pm * 4 * 112 + cg * 8 + w) * 64 + lane) * 16;
  u32 o_cs = OFF_CSG + (u32)((lane >> 5) * 3584 + ((lane >> 4) & 1) * F_ + 128 * cg + 16 * w + ((lane & 3) | ((lane & 8) >> 1) | ((lane & 4) << 1))) * 8;
  PIN(o_xp); PIN(xrdA); PIN(xrdB); PIN(xrdC); PIN(xrdD); PIN(o_s2); PIN(o_h); PIN(o_cs);
  if (w == 7) poll_s2<PSLEEP>(ws, xcd, lane);
  BAR();
  STAMP(8);
#pragma unroll
  for (int i = 0; i < 4; i++) DMA_X(i);
#pragma unroll
  for (int s = 0; s < 5; s++) W[14 + s] = trf(w3, s * 8192, s * 8192 + 512);
#pragma unroll
  for (int s = 0; s < 9; s++) W[19 + s] = rd_s1(s);
#define LD_ST2(tail) asm volatile("global_load_dwordx2 %0, %5, %7 sc1\n\tglobal_load_dwordx2 %1, %5, %7 offset:256 sc1\n\tglobal_load_dwordx2 %2, %5, %7 offset:512 sc1\n\tglobal_load_dwordx2 %3, %5, %7 offset:768 sc1\n\tglobal_load_dwordx2 %4, %6, %7 sc1" tail \
               : "=&v"(st2[0]), "=&v"(st2[1]), "=&v"(st2[2]), "=&v"(st2[3]), "=&v"(csg) : "v"(o_s2), "v"(o_cs), "s"(ws) : "memory")
  f32x16 a0 = {}, a1 = {}, a2 = {}, a3 = {};
  u32x4 XA[3], XB[3], X[4];
  {
    WAITV(3); BAR();
#pragma unroll
    for (int s = 0; s < 2; s++) { XA[s] = trf(xrdA, s * 2048, s * 2048 + 512); XB[s] = trf(xrdB, s * 2048, s * 2048 + 512); }
    static_for<0, 28>([&](auto S) {
      constexpr int s = decltype(S)::value;
      if constexpr (s + 2 < 28) {
        if constexpr (((s + 2) & 3) == 0) { WAITV(pp0_wait((s + 2) / 4)); BAR_RAW(); }
        XA[(s + 2) % 3] = trf(xrdA, (s + 2) * 2048, (s + 2) * 2048 + 512);
        XB[(s + 2) % 3] = trf(xrdB, (s + 2) * 2048, (s + 2) * 2048 + 512);
      } else {
        if constexpr (s == 26) { asm volatile("s_waitcnt vmcnt(3)" : "+v"(csg) :: "memory"); BAR_RAW(); }
        X[s - 26] = trf(xrdC, (s - 26) * 2048, (s - 26) * 2048 + 512);
        if constexpr (s == 27) X[2] = trf(xrdC, 4096, 4096 + 512);
        if constexpr (s == 26) {
#pragma unroll
          for (int m = 0; m < 4; m++) st2[m] = *(const u32x2*)(smem + LDS_ST2 + m * 256 + (lane & 31) * 8);
        }
      }
      a0 = __builtin_amdgcn_mfma_f32_32x32x16_bf16(asb(W[s]), asb(XA[s % 3]), a0, 0, 0, 0);
      a1 = __builtin_amdgcn_mfma_f32_32x32x16_bf16(asb(W[s]), asb(XB[s % 3]), a1, 0, 0, 0);
      PIN(a0); PIN(a1);
      if constexpr ((s & 1) == 0 && s <= 18) DMA_X(4 + s / 2);
      if constexpr ((s & 3) == 3 && s >= 7 && s <= 23) {
        constexpr int j = (s - 7) / 4;
        const char* bs_ = ws + ((23 + j) & 1) * 112 * 1024 + KROT((23 + j) >> 1); u32 l_ = lbase + LDS_W2 + (w * 5 + j) * 1024; DMAS(o_w2, bs_, l_);
      }
      if constexpr ((s & 3) == 1 && s >= 5 && s <= 25) LD_W2((s - 5) / 4);
      if constexpr (s == 20) {
        const u32 og = OFF_ST2G + (u32)(128 * pm + 16 * w) * 8 + lane * 16, l_ = lbase + LDS_ST2 + w * 128;
        asm volatile("s_mov_b32 m0, %2\n\ts_mov_b64 exec, 0xff\n\tglobal_load_lds_dwordx4 %0, %1 sc1\n\ts_mov_b64 exec, -1" :: "v"(og), "s"(ws), "s"(l_) : "memory");
        LD8S(csg, o_cs, ws);
      }
      SCHED();
    });
  }
  STAMP(23);
  auto fin = [&](f32x16& acc, int mt) {
    const u32 pl = st2[mt][0];
    const u32 e0 = lane < 32 ? ((pl ^ 0x8000u) & 0xffffu) : (pl >> 16);
    const u32x4 ex = {e0 | (e0 << 16), e0, 0u, 0u};
    acc = __builtin_amdgcn_mfma_f32_32x32x16_bf16(asb(Wx), asb(ex), acc, 0, 0, 0);
  };
  float ec1[4], esq[4];
  f32x2 ee, epr;
  u32x4 ho;
  auto stage = [&](const f32x16& acc, int mt, int j, int st) {
    if (st == 0) {
      const float a0_ = smul(acc[2 * j], ec1[mt]), a1_ = smul(acc[2 * j + 1], ec1[mt]);
      epr = (f32x2){smul(acc[2 * j], acc[8 + 2 * j]), smul(acc[2 * j + 1], acc[9 + 2 * j])};
      ee = (f32x2){__builtin_amdgcn_exp2f(a0_), __builtin_amdgcn_exp2f(a1_)};
      PIN(ee); PIN(epr);
    } else if (st == 1) {
      const float d0 = sfma(ee[0], esq[mt], esq[mt]), d1 = sfma(ee[1], esq[mt], esq[mt]);
      ee = (f32x2){__builtin_amdgcn_rcpf(d0), __builtin_amdgcn_rcpf(d1)};
      PIN(ee);
    } else {
      u32 v = pack2(smul(epr[0], ee[0]), smul(epr[1], ee[1])); PIN(v);
      ho[j] = v;
    }
  };
  auto put = [&](u32x4 o, int mt) { const char* bs = ws + (size_t)mt * (112 * 1024); ST16S(o_h, o, bs); };
  auto signal3 = [&](int half) {
    u32 old; const u32 c = lbase + LDS_CNT + 4 * (half == 2 ? 22 : half);
    asm volatile("s_mov_b64 exec, 1\n\tds_add_rtn_u32 %0, %1, %2\n\ts_mov_b64 exec, -1\n\ts_waitcnt lgkmcnt(0)" : "=&v"(old) : "v"(c), "v"(1u) : "memory");
    if (rfl(old) == 7u) {
      const u32 o = OFF_CTR + (u32)(128 + 3 * pm + half) * 128;
      asm volatile("s_mov_b64 exec, 1\n\tglobal_atomic_add %0, %1, %2\n\ts_mov_b64 exec, -1" :: "v"(o), "v"(1u), "s"(ws) : "memory");
    }
  };
  auto post3 = [&](int half) { lds_arrive(smem, 12 + half, lane); };
  auto wait3 = [&](bool ok, int half) {
    if (w == PW) {
      if (!ok) poll_eq14(ws, OFF_CTR + (u32)(128 + 3 * p2 + (half ? 1 + e2 : 0)) * 128);
      post3(half);
    } else if (!ok) lds_wait(smem, 12 + half, 1);
  };
#define LD_S3(half) do { if (w == PW) { const char* fb_ = ws + OFF_CTR + (128 + 3 * p2 + ((half) ? 1 + e2 : 0)) * 128; const u32 l_ = lbase + LDS_JUNK; \
    asm volatile("s_mov_b32 m0, %2\n\ts_nop 0\n\tglobal_load_lds_dword %0, %1 sc1" :: "v"(zero_off), "s"(fb_), "s"(l_) : "memory"); } } while (0)
#define S3_OK() (lds_peek(lbase + LDS_JUNK) == 14u)
  const u32 zero_off = 0;
  u32x4 Hf[8];
  bool pre = false; u32 s3w = 0;
  auto sig = [&](int half) {
    if (w == PW) {
      const u32 o = OFF_CTR + (u32)(128 + 3 * pm + half) * 128;
      asm volatile("s_mov_b64 exec, 1\n\tglobal_atomic_add %0, %1, %2\n\ts_mov_b64 exec, -1" :: "v"(o), "v"(1u), "s"(ws) : "memory");
    }
  };
  const u32 o_hl = OFF_H + (u32)(((4 * p2 + e2) * 112 + 14 * w) * 64 + lane) * 16;
  auto h_pre = [&](int s) {
    const char* bs = ws + KROT(s); u32 l_ = lbase + w * 7168 + s * 1024; DMAS(o_hl, bs, l_);
  };
  {
    auto item = [&](int k) {
      const int n = k / 3, st = k % 3;
      if (n < 4) stage(a0, 0, n, st); else if (n < 8) stage(a1, 1, n - 4, st); else stage(a2, 2, n - 8, st);
      if (st == 2 && (n & 3) == 3) put(ho, n >> 2);
    };
    static_for<0, 56>([&](auto S) {
      constexpr int u = decltype(S)::value, s = u % 28;
      if constexpr (u + 3 < 28) X[(u + 3) & 3] = trf(xrdC, (u + 3) * 2048, (u + 3) * 2048 + 512);
      else if constexpr (u + 3 < 56) X[(u + 3) & 3] = trf(xrdD, (u - 25) * 2048, (u - 25) * 2048 + 512);
      if constexpr (u < 28) { a2 = __builtin_amdgcn_mfma_f32_32x32x16_bf16(asb(W[s]), asb(X[u & 3]), a2, 0, 0, 0); PIN(a2); }
      else { a3 = __builtin_amdgcn_mfma_f32_32x32x16_bf16(asb(W[s]), asb(X[u & 3]), a3, 0, 0, 0); PIN(a3); }
      if constexpr (u == 0) {
        if (!__all(st2[0][1] == tag && st2[1][1] == tag && st2[2][1] == tag && st2[3][1] == tag && csg[1] == tag)) {
          WAITV(0);
#pragma nounroll
          for (int sp = 0; sp < SPIN_CAP; sp++) {
            __builtin_amdgcn_s_sleep(4);
            LD_ST2("\n\ts_waitcnt vmcnt(0)");
            if (__all(st2[0][1] == tag && st2[1][1] == tag && st2[2][1] == tag && st2[3][1] == tag && csg[1] == tag)) break;
          }
        }
        const float tot = __uint_as_float(csg[0]);
        const float h0 = rb(tot), h1 = rb(tot - h0), h2 = tot - h0 - h1;
        Wx = (u32x4){pack2(h0, h1), pack2(h2, 0.f), 0u, 0u};
        fin(a0, 0);
      }
      if constexpr (u == 1) {
        fin(a1, 1);
#pragma unroll
        for (int m = 0; m < 4; m++) { const float sd = hi_f(st2[m][0]); ec1[m] = -1.44269504088896341f * __builtin_amdgcn_rcpf(sd); esq[m] = sd * sd; }
      }
      if constexpr (u == 28) fin(a2, 2);
      if constexpr (DENS == 0 && u >= 3 && u < 21) {
        constexpr int k0 = 4 * ((u - 3) / 3) + (u - 3) % 3;
        item(k0);
        if constexpr ((u - 3) % 3 == 2) { SCHED(); item(k0 + 1); }
      }
      if constexpr (DENS == 1 && u >= 3 && u < 27) item(u - 3);
      if constexpr (u >= 31 && u < 43) item(24 + u - 31);
      if constexpr (u >= 2 && u < 28 && (u % 3) == 1) LD_W2(5 + u / 3);
      if constexpr (u >= 28 && u < 46 && (u & 1) == 0) LD_W2(14 + (u - 28) / 2);
      if constexpr (u == (DENS ? 39 : 35)) {
        WAITV(6);
        if (NOBAR) { lds_post(smem, 0); if (w == PW) lds_wait(smem, 0, 16); } else BAR_RAW();
        sig(0);
      }
      if constexpr (u == (DENS ? 50 : 46)) LD_S3(0);
      if constexpr (PRS && (u % (PRS ? PRS : 1)) == 0) __builtin_amdgcn_s_setprio(3 - ((u / (PRS ? PRS : 1)) & 3));
      SCHED();
    });
    if (PRS) __builtin_amdgcn_s_setprio(0);
    STAMP(28);
    fin(a3, 3);
    f32x2 e4[4], p4[4];
#pragma unroll
    for (int j = 0; j < 4; j++) {
      const f32x2 gg = {a3[2 * j], a3[2 * j + 1]}, uu = {a3[8 + 2 * j], a3[9 + 2 * j]};
      const f32x2 ar = gg * ec1[3];
      p4[j] = gg * uu;
      e4[j] = (f32x2){__builtin_amdgcn_exp2f(ar[0]), __builtin_amdgcn_exp2f(ar[1])};
    }
#pragma unroll
    for (int j = 0; j < 4; j++) {
      const f32x2 den = __builtin_elementwise_fma(e4[j], (f32x2){esq[3], esq[3]}, (f32x2){esq[3], esq[3]});
      ho[j] = pk2(p4[j] * (f32x2){__builtin_amdgcn_rcpf(den[0]), __builtin_amdgcn_rcpf(den[1])});
    }
    put(ho, 3);
  }
  STAMP(29);
  WAITV(1); BAR_RAW(); sig(1);
  pre = lds_peek(lbase + LDS_JUNK) == 14u;
  if (pre) {
#pragma unroll
    for (int s = 0; s < 4; s++) h_pre(s);
    WAITV(4);
  } else WAITV(0);
  signal3(2);
#pragma unroll
  for (int i = 0; i < 23; i++) PIN(W2[i]);
  if (!pre) {
    wait3(false, 0);
#pragma unroll
    for (int s = 0; s < 4; s++) h_pre(s);
  }
  STAMP(11);
#define LD_H(u) do { const char* bs_ = ws + ((u) / 14) * 2 * 112 * 1024 + KROT((u) % 14); LD16S(Hf[(u) & 7], o_hl, bs_); } while (0)
#pragma unroll
  for (int u = 4; u < BW; u++) LD_H(u);
#pragma unroll
  for (int j = 0; j < 5; j++) W2[23 + j] = *(const u32x4*)(smem + LDS_W2 + (w * 5 + j) * 1024 + lane * 16);
  const int ocol = 64 * dg + 32 * (w >> 2) + 8 * (w & 3) + 4 * (lane >> 5);
  const int orow = 128 * p2 + 32 * e2 + (lane & 31);
  u32 xres[8]; u32x4 gbg;
  const u32 o_gb = OFF_MODG + (u32)(5 * 224 + (ocol >> 1)) * 8;
  u32 rw0 = lbase + w * 1024 + lane * 16, rw1 = rw0 + 65536, rr0 = lbase + w * 8192 + lane * 16, rr1 = rr0 + 65536;
  PIN(rw0); PIN(rw1); PIN(rr0); PIN(rr1);
  auto dump = [&](u32 base, const f32x16& f, int o) {
    const int i0 = 4 * (o & 3);
    *(LAS f32x4*)(base + o * 8192) = (f32x4){f[i0], f[i0 + 1], f[i0 + 2], f[i0 + 3]};
  };
  auto gather = [&](u32 base) {
    f32x4 v[8];
#pragma unroll
    for (int i = 0; i < 8; i++) v[i] = *(const LAS f32x4*)(base + i * 1024);
    return ((v[0] + v[1]) + (v[2] + v[3])) + ((v[4] + v[5]) + (v[6] + v[7]));
  };
  f32x16 f0 = {}, f1 = {}, g0 = {}, g1 = {};
  f32x4 sum0;
  u32 hl = lbase + w * 7168 + lane * 16; PIN(hl);
  static_for<0, 28>([&](auto U) {
    constexpr int u = decltype(U)::value, s = u % 14, nx = u + BW;
    constexpr int cap = u <= CHK ? 13 : 27, last = nx - 1 > cap ? cap : nx - 1;
    if constexpr (u < 4) { WAITV(last - u); Hf[u] = *(const LAS u32x4*)(hl + u * 1024); }
    else asm volatile("s_waitcnt vmcnt(%1)" : "+v"(Hf[u & 7]) : "i"(last - u + (nx > 28 ? 9 : 0)) : "memory");
    if constexpr (u == CHK) {
      wait3(w == PW && S3_OK(), 1); STAMP(12);
#pragma unroll
      for (int q = 14; q < nx; q++) LD_H(q);
    }
    if constexpr (nx <= (u < CHK ? 13 : 27)) LD_H(nx);
    if constexpr (nx == CHK - 1) LD_S3(1);
    if constexpr (u < 14) {
      f0 = __builtin_amdgcn_mfma_f32_32x32x16_bf16(asb(W2[2 * s]), asb(Hf[u & 7]), f0, 0, 0, 0);
      f1 = __builtin_amdgcn_mfma_f32_32x32x16_bf16(asb(W2[2 * s + 1]), asb(Hf[u & 7]), f1, 0, 0, 0);
      PIN(f0); PIN(f1);
    } else {
      g0 = __builtin_amdgcn_mfma_f32_32x32x16_bf16(asb(W2[2 * s]), asb(Hf[u & 7]), g0, 0, 0, 0);
      if constexpr (u < 25) g1 = __builtin_amdgcn_mfma_f32_32x32x16_bf16(asb(W2[2 * s + 1]), asb(Hf[u & 7]), g1, 0, 0, 0);
      PIN(g0); PIN(g1);
    }
    if constexpr (u == 3) lds_post(smem, 15);
    if constexpr (nx == 28) {
      const u32 o = OFF_X1T + (u32)ocol * (L_ * 2) + orow * 2;
#pragma unroll
      for (int j = 0; j < 8; j++) { const char* bs = ws + (j & 3) * (L_ * 2) + (j >> 2) * 128; LD2S(xres[j], o, bs); }
      LD16S(gbg, o_gb, ws);
    }
    if constexpr (u == 14) { STAMP(30); lds_wait(smem, 15, 8); }
    if constexpr (u >= 15 && u < 19) { dump(rw0, f0, u - 15); dump(rw0, f1, 4 + u - 15); }
    if constexpr (u == 19) lds_post(smem, 17);
    if constexpr (u == 24) { lds_wait(smem, 17, 12); sum0 = gather(rr0); PIN(sum0); }
    SCHED();
  });
  static_for<25, 28>([&](auto U) {
    constexpr int u = decltype(U)::value, s = u % 14;
    g1 = __builtin_amdgcn_mfma_f32_32x32x16_bf16(asb(W2[2 * s + 1]), asb(Hf[u & 7]), g1, 0, 0, 0);
    PIN(g1);
    if constexpr (u == 26) { dump(rw1, g0, 0); dump(rw1, g0, 1); }
    if constexpr (u == 27) { dump(rw1, g0, 2); dump(rw1, g0, 3); }
    SCHED();
  });
  STAMP(13);
#pragma unroll
  for (int o = 4; o < 8; o++) dump(rw1, g1, o);
  asm volatile("s_waitcnt vmcnt(0)" : "+v"(xres[0]), "+v"(xres[1]), "+v"(xres[2]), "+v"(xres[3]), "+v"(xres[4]), "+v"(xres[5]), "+v"(xres[6]), "+v"(xres[7]), "+v"(gbg) :: "memory");
#pragma nounroll
  for (int sp = 0; sp < SPIN_CAP; sp++) {
    if (__all(gbg[1] == tag && gbg[3] == tag)) break;
    __builtin_amdgcn_s_sleep(4);
    asm volatile("global_load_dwordx4 %0, %1, %2 sc1\n\ts_waitcnt vmcnt(0)" : "=&v"(gbg) : "v"(o_gb), "s"(ws) : "memory");
  }
  auto finish = [&](f32x4 sum, int hf) {
    u32x2 res;
#pragma unroll
    for (int j = 0; j < 2; j++) {
      const f32x2 f = up2(pack2(sum[2 * j], sum[2 * j + 1]));
      const f32x2 m = up2(pk2(f * up2(gbg[2 * j])));
      res[j] = pk2((f32x2){lo_f(xres[4 * hf + 2 * j]), lo_f(xres[4 * hf + 2 * j + 1])} + m);
    }
    const u32 oo = (u32)((orow + 64 * hf) * D_ + ocol) * 2;
    if (OUTM == 0) asm volatile("global_store_dwordx2 %0, %1, %2" :: "v"(oo), "v"(res), "s"(p.out) : "memory");
    if (OUTM == 1) asm volatile("global_store_dwordx2 %0, %1, %2 sc1" :: "v"(oo), "v"(res), "s"(p.out) : "memory");
    if (OUTM == 2) asm volatile("global_store_dwordx2 %0, %1, %2 nt" :: "v"(oo), "v"(res), "s"(p.out) : "memory");
  };
  finish(sum0, 0);
  BAR();
  finish(gather(rr1), 1);
  STAMP(14);
  STAMP_OUT;
}

extern "C" void kernel_launch(void* const* d_in, const int* in_sizes, int n_in,
                              void* d_out, int out_size, void* d_ws, size_t ws_size,
                              hipStream_t stream) {
  Params p;
  p.x = (const u16*)d_in[0]; p.lar = (const u16*)d_in[1]; p.aim = (const u16*)d_in[2];
  p.cr = (const u16*)d_in[3]; p.ci = (const u16*)d_in[4]; p.ldt = (const u16*)d_in[5]; p.dsk = (const u16*)d_in[6];
  p.w1 = (const u16*)d_in[8]; p.w3 = (const u16*)d_in[9]; p.w2 = (const u16*)d_in[10];
  p.c = (const u16*)d_in[11]; p.wm = (const u16*)d_in[12]; p.ws = (char*)d_ws; p.out = (u16*)d_out;
  fused_kernel<<<256, 512, 0, stream>>>(p);
}
```
